```python
import torch

def _softmax(x, axis=-1):
    return torch.softmax(x, dim=axis)
def _gelu(x):
    return torch.nn.functional.gelu(x, approximate="tanh")
def _silu(x):
    return torch.nn.functional.silu(x)
def _sigmoid(x):
    return torch.sigmoid(x)
def _layernorm(x, axis=-1, eps=1e-5):
    m = x.mean(dim=axis, keepdim=True)
    v = x.var(dim=axis, keepdim=True, unbiased=False)
    return (x - m) / torch.sqrt(v + eps)
def _scatter_add(tgt, idx, src, axis=0):
    return tgt.clone().index_add_(axis, idx, src)
def _segment_sum(src, seg, n):
    out = torch.zeros((n,) + src.shape[1:], dtype=src.dtype)
    return out.index_add_(0, seg, src)
def _pad(x, pw):
    flat = tuple(v for pair in reversed(pw) for v in pair)
    return torch.nn.functional.pad(x, flat)

kernel_name = 'standalone__batchnorm__sampling'

def setup_inputs():

    _w = (lambda a: (lambda t: t.to(torch.bfloat16) if t.is_floating_point() else t)(torch.as_tensor(a)))
    B, S, C = 1024, 32, 512
    K = 3
    return {
        "x": _w((lambda *s: torch.randn(*s, dtype=torch.bfloat16) * 0.05)(B, S, C)),
        "w": _w((lambda *s: torch.randn(*s, dtype=torch.bfloat16) * 0.05)(K, C, C) * (1.0 / (K * C) ** 0.5)),
        "mean": _w((lambda *s: torch.randn(*s, dtype=torch.bfloat16) * 0.05)(C) * 0.1),
        "var": _w(torch.abs((lambda *s: torch.randn(*s, dtype=torch.bfloat16) * 0.05)(C)) + 0.5),
        "g": _w((lambda *s: torch.randn(*s, dtype=torch.bfloat16) * 0.05)(C) + 1.0),
        "b": _w((lambda *s: torch.randn(*s, dtype=torch.bfloat16) * 0.05)(C) * 0.1),
    }

def reference(x, w, mean, var, g, b):
    K = 3
    P = K // 2
    S = x.shape[1]
    xp = _pad(x, ((0, 0), (P, P), (0, 0)))
    y = sum(torch.einsum('bsc,cd->bsd', xp[:, k:k + S, :], w[k]) for k in range(K))
    inv_std = 1.0 / torch.sqrt(var + 1e-5)
    y = (y - mean) * (g * inv_std) + b
    return _silu(y)
```

```cpp
#include <hip/hip_runtime.h>
#include <hip/hip_bf16.h>
#include <stdint.h>

__global__ __launch_bounds__(256, 1) void conv_bn_silu_kernel(const void* x, const void* w, const void* mean, const void* var,
                                                             const void* g, const void* b, void* out, void* ws) {
  __shared__ __attribute__((aligned(16))) char smem[163840];
  unsigned tid = threadIdx.x;
  unsigned bid = blockIdx.x;
  unsigned lds = (unsigned)(uintptr_t)(__attribute__((address_space(3))) char*)smem;
  uint64_t karg = (uint64_t)__builtin_amdgcn_kernarg_segment_ptr();
  asm volatile(
"s_load_dwordx16 s[16:31], %[karg], 0x0\n"
"v_lshrrev_b32 v1, 6, v0\n"
"v_and_b32 v0, 63, v0\n"
"v_readfirstlane_b32 s60, v1\n"
"s_getpc_b64 s[92:93]\n"
"Lpf_start_%=:\n"
"s_and_b32 s93, s93, 0xffff\n"
"s_mov_b32 s94, Lpf_end_%=-Lpf_start_%=\n"
"s_mov_b32 s95, 0x20000\n"
"s_lshr_b32 s77, %[bid], 3\n"
"s_mul_i32 s78, s77, 8192\n"
"s_lshr_b32 s78, s78, 16\n"
"s_mul_i32 s78, s78, 8\n"
"s_sub_u32 s77, s77, s78\n"
"s_mul_i32 s77, s77, 4096\n"
"s_lshl_b32 s78, s60, 10\n"
"s_add_u32 s77, s77, s78\n"
"v_lshlrev_b32 v24, 4, v0\n"
"buffer_load_dwordx4 v[28:31], v24, s[92:95], s77 offen\n"
"s_lshr_b32 s77, %[bid], 3\n"
"s_and_b32 s84, s77, 3\n"
"s_lshr_b32 s61, s77, 2\n"
"s_and_b32 s78, %[bid], 7\n"
"s_lshl_b32 s78, s78, 3\n"
"s_add_u32 s85, s78, s61\n"
"s_lshl_b32 s85, s85, 19\n"
"s_and_b32 s61, s61, 0\n"
"s_mov_b32 s46, 524288\n"
"s_mov_b32 s50, 524288\n"
"s_mov_b32 s54, 524288\n"
"s_mov_b32 s58, 524288\n"
"s_mov_b32 s38, 262144\n"
"s_mov_b32 s42, 262144\n"
"s_mov_b32 s39, 0x20000\n"
"s_mov_b32 s43, 0x20000\n"
"s_mov_b32 s47, 0x20000\n"
"s_mov_b32 s51, 0x20000\n"
"s_mov_b32 s55, 0x20000\n"
"s_mov_b32 s59, 0x20000\n"
"s_lshl_b32 s68, s61, 7\n"
"s_lshl_b32 s76, s60, 10\n"
"s_add_u32 s77, s76, %[lds]\n"
"s_add_u32 s62, s77, 0\n"
"s_add_u32 s63, s77, 98304\n"
"v_lshrrev_b32 v8, 4, v0\n"
"v_bfe_u32 v9, v0, 2, 2\n"
"v_xor_b32 v9, v9, v8\n"
"v_lshlrev_b32 v9, 6, v9\n"
"v_and_b32 v10, 3, v0\n"
"v_lshl_or_b32 v9, v10, 4, v9\n"
"v_lshl_or_b32 v9, v8, 10, v9\n"
"s_lshl_b32 s77, s84, 8\n"
"s_lshl_b32 s78, s60, 12\n"
"s_add_u32 s77, s77, s78\n"
"v_add_u32 v4, s77, v9\n"
"v_and_b32 v9, 7, v0\n"
"v_xor_b32 v9, v9, v8\n"
"v_lshlrev_b32 v9, 4, v9\n"
"v_lshrrev_b32 v10, 3, v0\n"
"v_lshl_or_b32 v2, v10, 15, v9\n"
"v_lshlrev_b32 v25, 7, v0\n"
"v_cmp_gt_u32 vcc, 12, v0\n"
"v_mov_b32 v26, 0x40000000\n"
"v_cndmask_b32 v25, v26, v25, vcc\n"
"s_lshl_b32 s90, %[bid], 2\n"
"s_add_u32 s90, s90, s60\n"
"s_mul_i32 s90, s90, 1536\n"
"s_mov_b32 s66, 1572864\n"
"s_mov_b32 s67, 0x20000\n"
"s_waitcnt lgkmcnt(0)\n"
"s_mov_b32 s44, s18\n"
"s_and_b32 s45, s19, 0xffff\n"
"s_add_u32 s48, s44, 524288\n"
"s_addc_u32 s49, s45, 0\n"
"s_add_u32 s52, s44, 1048576\n"
"s_addc_u32 s53, s45, 0\n"
"s_lshl_b32 s77, s68, 9\n"
"s_add_u32 s78, s77, 16384\n"
"s_add_u32 m0, s62, 0\n"
"s_nop 0\n"
"buffer_load_dwordx4 v4, s[44:47], s77 offen lds\n"
"s_add_u32 m0, s62, 4096\n"
"s_nop 0\n"
"buffer_load_dwordx4 v4, s[44:47], s78 offen lds\n"
"s_add_u32 m0, s62, 8192\n"
"s_nop 0\n"
"buffer_load_dwordx4 v4, s[48:51], s77 offen lds\n"
"s_add_u32 m0, s62, 12288\n"
"s_nop 0\n"
"buffer_load_dwordx4 v4, s[48:51], s78 offen lds\n"
"s_add_u32 m0, s62, 16384\n"
"s_nop 0\n"
"buffer_load_dwordx4 v4, s[52:55], s77 offen lds\n"
"s_add_u32 m0, s62, 20480\n"
"s_nop 0\n"
"buffer_load_dwordx4 v4, s[52:55], s78 offen lds\n"
"s_add_u32 s36, s16, s85\n"
"s_addc_u32 s37, s17, 0\n"
"s_and_b32 s37, s37, 0xffff\n"
"s_add_u32 s40, s36, 262144\n"
"s_addc_u32 s41, s37, 0\n"
"s_add_u32 s77, s68, s76\n"
"s_add_u32 s78, s77, 4096\n"
"s_add_u32 m0, s63, 0\n"
"s_nop 0\n"
"buffer_load_dwordx4 v2, s[36:39], s77 offen nt lds\n"
"s_add_u32 m0, s63, 8256\n"
"s_nop 0\n"
"buffer_load_dwordx4 v2, s[40:43], s77 offen nt lds\n"
"s_add_u32 m0, s63, 4096\n"
"s_nop 0\n"
"buffer_load_dwordx4 v2, s[36:39], s78 offen nt lds\n"
"s_add_u32 m0, s63, 12352\n"
"s_nop 0\n"
"buffer_load_dwordx4 v2, s[40:43], s78 offen nt lds\n"
"s_mov_b32 s64, s44\n"
"s_mov_b32 s65, s45\n"
"buffer_load_dword v27, v25, s[64:67], s90 offen\n"
"s_lshl_b32 s77, s68, 9\n"
"s_add_u32 s77, s77, 32768\n"
"s_add_u32 s78, s77, 16384\n"
"s_add_u32 m0, s62, 24576\n"
"s_nop 0\n"
"buffer_load_dwordx4 v4, s[44:47], s77 offen lds\n"
"s_add_u32 m0, s62, 28672\n"
"s_nop 0\n"
"buffer_load_dwordx4 v4, s[44:47], s78 offen lds\n"
"s_add_u32 m0, s62, 32768\n"
"s_nop 0\n"
"buffer_load_dwordx4 v4, s[48:51], s77 offen lds\n"
"s_add_u32 m0, s62, 36864\n"
"s_nop 0\n"
"buffer_load_dwordx4 v4, s[48:51], s78 offen lds\n"
"s_add_u32 m0, s62, 40960\n"
"s_nop 0\n"
"buffer_load_dwordx4 v4, s[52:55], s77 offen lds\n"
"s_add_u32 m0, s62, 45056\n"
"s_nop 0\n"
"buffer_load_dwordx4 v4, s[52:55], s78 offen lds\n"
"s_add_u32 s56, s28, s85\n"
"s_addc_u32 s57, s29, 0\n"
"s_and_b32 s57, s57, 0xffff\n"
"s_add_u32 s77, s61, 1\n"
"s_and_b32 s77, s77, 7\n"
"s_lshl_b32 s69, s77, 7\n"
"s_add_u32 s77, s61, 2\n"
"s_and_b32 s77, s77, 7\n"
"s_lshl_b32 s70, s77, 7\n"
"s_add_u32 s77, s61, 3\n"
"s_and_b32 s77, s77, 7\n"
"s_lshl_b32 s71, s77, 7\n"
"s_add_u32 s77, s61, 4\n"
"s_and_b32 s77, s77, 7\n"
"s_lshl_b32 s72, s77, 7\n"
"s_add_u32 s77, s61, 5\n"
"s_and_b32 s77, s77, 7\n"
"s_lshl_b32 s73, s77, 7\n"
"s_add_u32 s77, s61, 6\n"
"s_and_b32 s77, s77, 7\n"
"s_lshl_b32 s74, s77, 7\n"
"s_add_u32 s77, s61, 7\n"
"s_and_b32 s77, s77, 7\n"
"s_lshl_b32 s75, s77, 7\n"
"s_lshl_b32 s77, s84, 8\n"
"s_lshl_b32 s78, s60, 6\n"
"s_add_u32 s77, s77, s78\n"
"v_lshlrev_b32 v9, 4, v8\n"
"v_add_u32 v248, s77, v9\n"
"v_and_b32 v9, 15, v0\n"
"v_lshl_add_u32 v3, v9, 15, v248\n"
"s_lshl_b32 s77, s60, 1\n"
"s_add_u32 s77, s77, s61\n"
"s_add_u32 s77, s77, 0\n"
"s_and_b32 s77, s77, 7\n"
"s_lshl_b32 s79, s77, 7\n"
"s_add_u32 s79, s79, 8192\n"
"s_lshl_b32 s77, s60, 1\n"
"s_add_u32 s77, s77, s61\n"
"s_add_u32 s77, s77, 1\n"
"s_and_b32 s77, s77, 7\n"
"s_lshl_b32 s80, s77, 7\n"
"s_add_u32 s80, s80, 8192\n"
"s_lshl_b32 s77, s60, 11\n"
"s_add_u32 s81, s77, %[lds]\n"
"s_mov_b32 s83, 0xbfb8aa3b\n"
"s_mov_b32 s82, 0\n"
"v_bfe_u32 v10, v0, 2, 2\n"
"v_xor_b32 v11, s60, v10\n"
"v_lshlrev_b32 v11, 6, v11\n"
"v_lshl_add_u32 v10, v8, 3, v10\n"
"v_lshl_or_b32 v11, v10, 8, v11\n"
"v_and_b32 v10, 3, v0\n"
"v_lshl_or_b32 v11, v10, 4, v11\n"
"s_add_u32 s77, %[lds], 0\n"
"v_add_u32 v5, s77, v11\n"
"v_add_u32 v6, 49152, v5\n"
"v_bfe_u32 v10, v0, 1, 2\n"
"v_xor_b32 v10, v10, v8\n"
"v_lshlrev_b32 v10, 4, v10\n"
"v_and_b32 v11, 7, v0\n"
"v_lshl_or_b32 v10, v11, 7, v10\n"
"v_bfe_u32 v11, v0, 3, 1\n"
"v_mul_u32_u24 v12, 8256, v11\n"
"v_mul_u32_u24 v13, 8256, v11\n"
"s_add_u32 s77, %[lds], 98304\n"
"v_add3_u32 v7, v12, v10, s77\n"
"v_add3_u32 v1, v13, v10, %[lds]\n"
"v_mov_b32 v0, v1\n"
"global_load_dwordx4 v[232:235], v248, s[20:21]\n"
"global_load_dwordx4 v[236:239], v248, s[22:23]\n"
"global_load_dwordx4 v[240:243], v248, s[24:25]\n"
"global_load_dwordx4 v[244:247], v248, s[26:27]\n"
"s_waitcnt vmcnt(11)\n"
"s_barrier\n"
"ds_read_b64_tr_b16 a[0:1], v5 offset:0\n"
"ds_read_b64_tr_b16 a[2:3], v5 offset:1024\n"
"ds_read_b64_tr_b16 a[4:5], v5 offset:8\n"
"ds_read_b64_tr_b16 a[6:7], v5 offset:1032\n"
"ds_read_b64_tr_b16 a[8:9], v5 offset:8192\n"
"ds_read_b64_tr_b16 a[10:11], v5 offset:9216\n"
"ds_read_b64_tr_b16 a[12:13], v5 offset:8200\n"
"ds_read_b64_tr_b16 a[14:15], v5 offset:9224\n"
"ds_read_b64_tr_b16 a[16:17], v5 offset:16384\n"
"ds_read_b64_tr_b16 a[18:19], v5 offset:17408\n"
"ds_read_b64_tr_b16 a[20:21], v5 offset:16392\n"
"ds_read_b64_tr_b16 a[22:23], v5 offset:17416\n"
"ds_read_b128 v[32:35], v7 offset:0\n"
"ds_read_b128 v[36:39], v7 offset:1024\n"
"ds_read_b128 v[40:43], v7 offset:2048\n"
"s_lshl_b32 s77, s69, 9\n"
"s_add_u32 s78, s77, 16384\n"
"s_add_u32 m0, s62, 49152\n"
"s_nop 0\n"
"buffer_load_dwordx4 v4, s[44:47], s77 offen lds\n"
"s_add_u32 m0, s62, 53248\n"
"s_nop 0\n"
"buffer_load_dwordx4 v4, s[44:47], s78 offen lds\n"
"s_add_u32 m0, s62, 57344\n"
"s_nop 0\n"
"buffer_load_dwordx4 v4, s[48:51], s77 offen lds\n"
"s_add_u32 m0, s62, 61440\n"
"s_nop 0\n"
"buffer_load_dwordx4 v4, s[48:51], s78 offen lds\n"
"s_add_u32 m0, s62, 65536\n"
"s_nop 0\n"
"buffer_load_dwordx4 v4, s[52:55], s77 offen lds\n"
"s_add_u32 m0, s62, 69632\n"
"s_nop 0\n"
"buffer_load_dwordx4 v4, s[52:55], s78 offen lds\n"
"s_add_u32 s77, s69, s76\n"
"s_add_u32 s78, s77, 4096\n"
"s_add_u32 m0, s63, 16448\n"
"s_nop 0\n"
"buffer_load_dwordx4 v2, s[36:39], s77 offen nt lds\n"
"s_add_u32 m0, s63, 24704\n"
"s_nop 0\n"
"buffer_load_dwordx4 v2, s[40:43], s77 offen nt lds\n"
"s_add_u32 m0, s63, 20544\n"
"s_nop 0\n"
"buffer_load_dwordx4 v2, s[36:39], s78 offen nt lds\n"
"s_add_u32 m0, s63, 28800\n"
"s_nop 0\n"
"buffer_load_dwordx4 v2, s[40:43], s78 offen nt lds\n"
"s_waitcnt vmcnt(14) lgkmcnt(3)\n"
"s_barrier\n"
"s_waitcnt lgkmcnt(2)\n"
"v_mfma_f32_16x16x32_bf16 v[56:59], a[0:3], v[32:35], 0\n"
"ds_read_b128 v[44:47], v7 offset:3072\n"
"ds_read_b64_tr_b16 a[24:25], v5 offset:24576\n"
"v_mfma_f32_16x16x32_bf16 v[60:63], a[4:7], v[32:35], 0\n"
"s_lshl_b32 s77, s69, 9\n"
"s_add_u32 s77, s77, 32768\n"
"v_mfma_f32_16x16x32_bf16 v[48:51], a[8:11], v[32:35], 0\n"
"s_add_u32 s78, s77, 16384\n"
"s_add_u32 m0, s62, 73728\n"
"v_mfma_f32_16x16x32_bf16 v[52:55], a[12:15], v[32:35], 0\n"
"ds_read_b64_tr_b16 a[26:27], v5 offset:25600\n"
"s_waitcnt lgkmcnt(4)\n"
"v_mfma_f32_16x16x32_bf16 v[64:67], a[0:3], v[36:39], 0\n"
"ds_read_b128 v[32:35], v7 offset:4096\n"
"v_mfma_f32_16x16x32_bf16 v[68:71], a[4:7], v[36:39], 0\n"
"buffer_load_dwordx4 v4, s[44:47], s77 offen lds\n"
"v_mfma_f32_16x16x32_bf16 v[56:59], a[8:11], v[36:39], v[56:59]\n"
"ds_read_b64_tr_b16 a[28:29], v5 offset:24584\n"
"s_add_u32 m0, s62, 77824\n"
"v_mfma_f32_16x16x32_bf16 v[60:63], a[12:15], v[36:39], v[60:63]\n"
"buffer_load_dwordx4 v4, s[44:47], s78 offen lds\n"
"v_mfma_f32_16x16x32_bf16 v[48:51], a[16:19], v[36:39], v[48:51]\n"
"s_add_u32 m0, s62, 81920\n"
"v_mfma_f32_16x16x32_bf16 v[52:55], a[20:23], v[36:39], v[52:55]\n"
"ds_read_b64_tr_b16 a[30:31], v5 offset:25608\n"
"s_waitcnt lgkmcnt(6)\n"
"v_mfma_f32_16x16x32_bf16 v[72:75], a[0:3], v[40:43], 0\n"
"ds_read_b128 v[36:39], v7 offset:5120\n"
"v_mfma_f32_16x16x32_bf16 v[76:79], a[4:7], v[40:43], 0\n"
"buffer_load_dwordx4 v4, s[48:51], s77 offen lds\n"
"v_mfma_f32_16x16x32_bf16 v[64:67], a[8:11], v[40:43], v[64:67]\n"
"ds_read_b64_tr_b16 a[32:33], v5 offset:32768\n"
"s_add_u32 m0, s62, 86016\n"
"v_mfma_f32_16x16x32_bf16 v[68:71], a[12:15], v[40:43], v[68:71]\n"
"buffer_load_dwordx4 v4, s[48:51], s78 offen lds\n"
"v_mfma_f32_16x16x32_bf16 v[56:59], a[16:19], v[40:43], v[56:59]\n"
"s_add_u32 m0, s62, 90112\n"
"v_mfma_f32_16x16x32_bf16 v[60:63], a[20:23], v[40:43], v[60:63]\n"
"ds_read_b64_tr_b16 a[34:35], v5 offset:33792\n"
"s_waitcnt lgkmcnt(8)\n"
"v_mfma_f32_16x16x32_bf16 v[80:83], a[0:3], v[44:47], 0\n"
"ds_read_b128 v[40:43], v7 offset:6144\n"
"v_mfma_f32_16x16x32_bf16 v[84:87], a[4:7], v[44:47], 0\n"
"buffer_load_dwordx4 v4, s[52:55], s77 offen lds\n"
"v_mfma_f32_16x16x32_bf16 v[72:75], a[8:11], v[44:47], v[72:75]\n"
"ds_read_b64_tr_b16 a[36:37], v5 offset:32776\n"
"s_add_u32 m0, s62, 94208\n"
"v_mfma_f32_16x16x32_bf16 v[76:79], a[12:15], v[44:47], v[76:79]\n"
"buffer_load_dwordx4 v4, s[52:55], s78 offen lds\n"
"v_mfma_f32_16x16x32_bf16 v[64:67], a[16:19], v[44:47], v[64:67]\n"
"s_add_u32 s77, s70, s76\n"
"s_add_u32 s78, s77, 4096\n"
"v_mfma_f32_16x16x32_bf16 v[68:71], a[20:23], v[44:47], v[68:71]\n"
"ds_read_b64_tr_b16 a[38:39], v5 offset:33800\n"
"s_waitcnt lgkmcnt(8)\n"
"v_mfma_f32_16x16x32_bf16 v[88:91], a[0:3], v[32:35], 0\n"
"ds_read_b128 v[44:47], v7 offset:7168\n"
"s_add_u32 m0, s63, 32896\n"
"v_mfma_f32_16x16x32_bf16 v[92:95], a[4:7], v[32:35], 0\n"
"buffer_load_dwordx4 v2, s[36:39], s77 offen nt lds\n"
"v_mfma_f32_16x16x32_bf16 v[80:83], a[8:11], v[32:35], v[80:83]\n"
"ds_read_b64_tr_b16 a[40:41], v5 offset:40960\n"
"s_add_u32 m0, s63, 41152\n"
"v_mfma_f32_16x16x32_bf16 v[84:87], a[12:15], v[32:35], v[84:87]\n"
"buffer_load_dwordx4 v2, s[40:43], s77 offen nt lds\n"
"v_mfma_f32_16x16x32_bf16 v[72:75], a[16:19], v[32:35], v[72:75]\n"
"s_add_u32 m0, s63, 36992\n"
"v_mfma_f32_16x16x32_bf16 v[76:79], a[20:23], v[32:35], v[76:79]\n"
"ds_read_b64_tr_b16 a[42:43], v5 offset:41984\n"
"s_waitcnt lgkmcnt(8)\n"
"v_mfma_f32_16x16x32_bf16 v[96:99], a[0:3], v[36:39], 0\n"
"ds_read_b128 v[32:35], v7 offset:64\n"
"v_mfma_f32_16x16x32_bf16 v[100:103], a[4:7], v[36:39], 0\n"
"buffer_load_dwordx4 v2, s[36:39], s78 offen nt lds\n"
"v_mfma_f32_16x16x32_bf16 v[88:91], a[8:11], v[36:39], v[88:91]\n"
"ds_read_b64_tr_b16 a[44:45], v5 offset:40968\n"
"s_add_u32 m0, s63, 45248\n"
"v_mfma_f32_16x16x32_bf16 v[92:95], a[12:15], v[36:39], v[92:95]\n"
"buffer_load_dwordx4 v2, s[40:43], s78 offen nt lds\n"
"v_mfma_f32_16x16x32_bf16 v[80:83], a[16:19], v[36:39], v[80:83]\n"
"s_lshl_b32 s77, s70, 9\n"
"s_add_u32 s78, s77, 16384\n"
"v_mfma_f32_16x16x32_bf16 v[84:87], a[20:23], v[36:39], v[84:87]\n"
"ds_read_b64_tr_b16 a[46:47], v5 offset:41992\n"
"s_waitcnt lgkmcnt(8)\n"
"v_mfma_f32_16x16x32_bf16 v[104:107], a[0:3], v[40:43], 0\n"
"ds_read_b128 v[36:39], v7 offset:1088\n"
"s_add_u32 m0, s62, 0\n"
"v_mfma_f32_16x16x32_bf16 v[108:111], a[4:7], v[40:43], 0\n"
"buffer_load_dwordx4 v4, s[44:47], s77 offen lds\n"
"v_mfma_f32_16x16x32_bf16 v[96:99], a[8:11], v[40:43], v[96:99]\n"
"s_add_u32 m0, s62, 4096\n"
"v_mfma_f32_16x16x32_bf16 v[100:103], a[12:15], v[40:43], v[100:103]\n"
"buffer_load_dwordx4 v4, s[44:47], s78 offen lds\n"
"v_mfma_f32_16x16x32_bf16 v[88:91], a[16:19], v[40:43], v[88:91]\n"
"s_add_u32 m0, s62, 8192\n"
"v_mfma_f32_16x16x32_bf16 v[92:95], a[20:23], v[40:43], v[92:95]\n"
"s_waitcnt lgkmcnt(6)\n"
"v_mfma_f32_16x16x32_bf16 v[112:115], a[0:3], v[44:47], 0\n"
"ds_read_b128 v[40:43], v7 offset:2112\n"
"v_mfma_f32_16x16x32_bf16 v[116:119], a[4:7], v[44:47], 0\n"
"buffer_load_dwordx4 v4, s[48:51], s77 offen lds\n"
"v_mfma_f32_16x16x32_bf16 v[104:107], a[8:11], v[44:47], v[104:107]\n"
"s_add_u32 m0, s62, 12288\n"
"v_mfma_f32_16x16x32_bf16 v[108:111], a[12:15], v[44:47], v[108:111]\n"
"buffer_load_dwordx4 v4, s[48:51], s78 offen lds\n"
"v_mfma_f32_16x16x32_bf16 v[96:99], a[16:19], v[44:47], v[96:99]\n"
"s_add_u32 m0, s62, 16384\n"
"s_nop 0\n"
"buffer_load_dwordx4 v4, s[52:55], s77 offen lds\n"
"v_mfma_f32_16x16x32_bf16 v[100:103], a[20:23], v[44:47], v[100:103]\n"
"s_add_u32 m0, s62, 20480\n"
"s_nop 0\n"
"buffer_load_dwordx4 v4, s[52:55], s78 offen lds\n"
"s_waitcnt vmcnt(16) lgkmcnt(2)\n"
"s_barrier\n"
"v_mfma_f32_16x16x32_bf16 v[56:59], a[24:27], v[32:35], v[56:59]\n"
"ds_read_b128 v[44:47], v7 offset:3136\n"
"ds_read_b64_tr_b16 a[48:49], v6 offset:0\n"
"v_mfma_f32_16x16x32_bf16 v[60:63], a[28:31], v[32:35], v[60:63]\n"
"s_lshl_b32 s77, s70, 9\n"
"v_mfma_f32_16x16x32_bf16 v[48:51], a[32:35], v[32:35], v[48:51]\n"
"s_add_u32 s77, s77, 32768\n"
"v_mfma_f32_16x16x32_bf16 v[52:55], a[36:39], v[32:35], v[52:55]\n"
"ds_read_b64_tr_b16 a[50:51], v6 offset:1024\n"
"s_waitcnt lgkmcnt(4)\n"
"v_mfma_f32_16x16x32_bf16 v[64:67], a[24:27], v[36:39], v[64:67]\n"
"ds_read_b128 v[32:35], v7 offset:4160\n"
"v_mfma_f32_16x16x32_bf16 v[68:71], a[28:31], v[36:39], v[68:71]\n"
"s_add_u32 s78, s77, 16384\n"
"v_mfma_f32_16x16x32_bf16 v[56:59], a[32:35], v[36:39], v[56:59]\n"
"ds_read_b64_tr_b16 a[52:53], v6 offset:8\n"
"v_mfma_f32_16x16x32_bf16 v[60:63], a[36:39], v[36:39], v[60:63]\n"
"s_add_u32 m0, s62, 24576\n"
"v_mfma_f32_16x16x32_bf16 v[48:51], a[40:43], v[36:39], v[48:51]\n"
"buffer_load_dwordx4 v4, s[44:47], s77 offen lds\n"
"v_mfma_f32_16x16x32_bf16 v[52:55], a[44:47], v[36:39], v[52:55]\n"
"ds_read_b64_tr_b16 a[54:55], v6 offset:1032\n"
"s_waitcnt lgkmcnt(6)\n"
"v_mfma_f32_16x16x32_bf16 v[72:75], a[24:27], v[40:43], v[72:75]\n"
"ds_read_b128 v[36:39], v7 offset:5184\n"
"v_mfma_f32_16x16x32_bf16 v[76:79], a[28:31], v[40:43], v[76:79]\n"
"v_mfma_f32_16x16x32_bf16 v[64:67], a[32:35], v[40:43], v[64:67]\n"
"ds_read_b64_tr_b16 a[56:57], v6 offset:8192\n"
"v_mfma_f32_16x16x32_bf16 v[68:71], a[36:39], v[40:43], v[68:71]\n"
"s_add_u32 m0, s62, 28672\n"
"v_mfma_f32_16x16x32_bf16 v[56:59], a[40:43], v[40:43], v[56:59]\n"
"buffer_load_dwordx4 v4, s[44:47], s78 offen lds\n"
"v_mfma_f32_16x16x32_bf16 v[60:63], a[44:47], v[40:43], v[60:63]\n"
"ds_read_b64_tr_b16 a[58:59], v6 offset:9216\n"
"s_waitcnt lgkmcnt(8)\n"
"v_mfma_f32_16x16x32_bf16 v[80:83], a[24:27], v[44:47], v[80:83]\n"
"ds_read_b128 v[40:43], v7 offset:6208\n"
"v_mfma_f32_16x16x32_bf16 v[84:87], a[28:31], v[44:47], v[84:87]\n"
"v_mfma_f32_16x16x32_bf16 v[72:75], a[32:35], v[44:47], v[72:75]\n"
"ds_read_b64_tr_b16 a[60:61], v6 offset:8200\n"
"v_mfma_f32_16x16x32_bf16 v[76:79], a[36:39], v[44:47], v[76:79]\n"
"s_add_u32 m0, s62, 32768\n"
"v_mfma_f32_16x16x32_bf16 v[64:67], a[40:43], v[44:47], v[64:67]\n"
"buffer_load_dwordx4 v4, s[48:51], s77 offen lds\n"
"v_mfma_f32_16x16x32_bf16 v[68:71], a[44:47], v[44:47], v[68:71]\n"
"ds_read_b64_tr_b16 a[62:63], v6 offset:9224\n"
"s_waitcnt lgkmcnt(8)\n"
"v_mfma_f32_16x16x32_bf16 v[88:91], a[24:27], v[32:35], v[88:91]\n"
"ds_read_b128 v[44:47], v7 offset:7232\n"
"v_mfma_f32_16x16x32_bf16 v[92:95], a[28:31], v[32:35], v[92:95]\n"
"v_mfma_f32_16x16x32_bf16 v[80:83], a[32:35], v[32:35], v[80:83]\n"
"ds_read_b64_tr_b16 a[64:65], v6 offset:16384\n"
"v_mfma_f32_16x16x32_bf16 v[84:87], a[36:39], v[32:35], v[84:87]\n"
"s_add_u32 m0, s62, 36864\n"
"v_mfma_f32_16x16x32_bf16 v[72:75], a[40:43], v[32:35], v[72:75]\n"
"buffer_load_dwordx4 v4, s[48:51], s78 offen lds\n"
"v_mfma_f32_16x16x32_bf16 v[76:79], a[44:47], v[32:35], v[76:79]\n"
"ds_read_b64_tr_b16 a[66:67], v6 offset:17408\n"
"s_waitcnt lgkmcnt(8)\n"
"v_mfma_f32_16x16x32_bf16 v[96:99], a[24:27], v[36:39], v[96:99]\n"
"ds_read_b128 v[32:35], v7 offset:16448\n"
"v_mfma_f32_16x16x32_bf16 v[100:103], a[28:31], v[36:39], v[100:103]\n"
"v_mfma_f32_16x16x32_bf16 v[88:91], a[32:35], v[36:39], v[88:91]\n"
"ds_read_b64_tr_b16 a[68:69], v6 offset:16392\n"
"v_mfma_f32_16x16x32_bf16 v[92:95], a[36:39], v[36:39], v[92:95]\n"
"s_add_u32 m0, s62, 40960\n"
"v_mfma_f32_16x16x32_bf16 v[80:83], a[40:43], v[36:39], v[80:83]\n"
"v_mfma_f32_16x16x32_bf16 v[84:87], a[44:47], v[36:39], v[84:87]\n"
"ds_read_b64_tr_b16 a[70:71], v6 offset:17416\n"
"s_waitcnt lgkmcnt(8)\n"
"v_mfma_f32_16x16x32_bf16 v[104:107], a[24:27], v[40:43], v[104:107]\n"
"ds_read_b128 v[36:39], v7 offset:17472\n"
"v_mfma_f32_16x16x32_bf16 v[108:111], a[28:31], v[40:43], v[108:111]\n"
"buffer_load_dwordx4 v4, s[52:55], s77 offen lds\n"
"v_mfma_f32_16x16x32_bf16 v[96:99], a[32:35], v[40:43], v[96:99]\n"
"v_mfma_f32_16x16x32_bf16 v[100:103], a[36:39], v[40:43], v[100:103]\n"
"s_add_u32 m0, s62, 45056\n"
"v_mfma_f32_16x16x32_bf16 v[88:91], a[40:43], v[40:43], v[88:91]\n"
"buffer_load_dwordx4 v4, s[52:55], s78 offen lds\n"
"v_mfma_f32_16x16x32_bf16 v[92:95], a[44:47], v[40:43], v[92:95]\n"
"s_waitcnt lgkmcnt(6)\n"
"v_mfma_f32_16x16x32_bf16 v[112:115], a[24:27], v[44:47], v[112:115]\n"
"ds_read_b128 v[40:43], v7 offset:18496\n"
"v_mfma_f32_16x16x32_bf16 v[116:119], a[28:31], v[44:47], v[116:119]\n"
"v_mfma_f32_16x16x32_bf16 v[104:107], a[32:35], v[44:47], v[104:107]\n"
"v_mfma_f32_16x16x32_bf16 v[108:111], a[36:39], v[44:47], v[108:111]\n"
"v_mfma_f32_16x16x32_bf16 v[96:99], a[40:43], v[44:47], v[96:99]\n"
"v_mfma_f32_16x16x32_bf16 v[100:103], a[44:47], v[44:47], v[100:103]\n"
"s_waitcnt vmcnt(16) lgkmcnt(2)\n"
"s_barrier\n"
"v_mfma_f32_16x16x32_bf16 v[56:59], a[48:51], v[32:35], v[56:59]\n"
"ds_read_b128 v[44:47], v7 offset:19520\n"
"ds_read_b64_tr_b16 a[72:73], v6 offset:24576\n"
"v_mfma_f32_16x16x32_bf16 v[60:63], a[52:55], v[32:35], v[60:63]\n"
"s_lshl_b32 s77, s71, 9\n"
"s_add_u32 s78, s77, 16384\n"
"v_mfma_f32_16x16x32_bf16 v[48:51], a[56:59], v[32:35], v[48:51]\n"
"s_add_u32 m0, s62, 49152\n"
"v_mfma_f32_16x16x32_bf16 v[52:55], a[60:63], v[32:35], v[52:55]\n"
"ds_read_b64_tr_b16 a[74:75], v6 offset:25600\n"
"s_waitcnt lgkmcnt(4)\n"
"v_mfma_f32_16x16x32_bf16 v[64:67], a[48:51], v[36:39], v[64:67]\n"
"ds_read_b128 v[32:35], v7 offset:20544\n"
"v_mfma_f32_16x16x32_bf16 v[68:71], a[52:55], v[36:39], v[68:71]\n"
"buffer_load_dwordx4 v4, s[44:47], s77 offen lds\n"
"v_mfma_f32_16x16x32_bf16 v[56:59], a[56:59], v[36:39], v[56:59]\n"
"ds_read_b64_tr_b16 a[76:77], v6 offset:24584\n"
"v_mfma_f32_16x16x32_bf16 v[60:63], a[60:63], v[36:39], v[60:63]\n"
"s_add_u32 m0, s62, 53248\n"
"v_mfma_f32_16x16x32_bf16 v[48:51], a[64:67], v[36:39], v[48:51]\n"
"buffer_load_dwordx4 v4, s[44:47], s78 offen lds\n"
"v_mfma_f32_16x16x32_bf16 v[52:55], a[68:71], v[36:39], v[52:55]\n"
"ds_read_b64_tr_b16 a[78:79], v6 offset:25608\n"
"s_waitcnt lgkmcnt(6)\n"
"v_mfma_f32_16x16x32_bf16 v[72:75], a[48:51], v[40:43], v[72:75]\n"
"ds_read_b128 v[36:39], v7 offset:21568\n"
"v_mfma_f32_16x16x32_bf16 v[76:79], a[52:55], v[40:43], v[76:79]\n"
"s_add_u32 m0, s62, 57344\n"
"v_mfma_f32_16x16x32_bf16 v[64:67], a[56:59], v[40:43], v[64:67]\n"
"ds_read_b64_tr_b16 a[80:81], v6 offset:32768\n"
"v_mfma_f32_16x16x32_bf16 v[68:71], a[60:63], v[40:43], v[68:71]\n"
"buffer_load_dwordx4 v4, s[48:51], s77 offen lds\n"
"v_mfma_f32_16x16x32_bf16 v[56:59], a[64:67], v[40:43], v[56:59]\n"
"s_add_u32 m0, s62, 61440\n"
"v_mfma_f32_16x16x32_bf16 v[60:63], a[68:71], v[40:43], v[60:63]\n"
"ds_read_b64_tr_b16 a[82:83], v6 offset:33792\n"
"s_waitcnt lgkmcnt(8)\n"
"v_mfma_f32_16x16x32_bf16 v[80:83], a[48:51], v[44:47], v[80:83]\n"
"ds_read_b128 v[40:43], v7 offset:22592\n"
"v_mfma_f32_16x16x32_bf16 v[84:87], a[52:55], v[44:47], v[84:87]\n"
"buffer_load_dwordx4 v4, s[48:51], s78 offen lds\n"
"v_mfma_f32_16x16x32_bf16 v[72:75], a[56:59], v[44:47], v[72:75]\n"
"ds_read_b64_tr_b16 a[84:85], v6 offset:32776\n"
"s_add_u32 m0, s62, 65536\n"
"v_mfma_f32_16x16x32_bf16 v[76:79], a[60:63], v[44:47], v[76:79]\n"
"buffer_load_dwordx4 v4, s[52:55], s77 offen lds\n"
"v_mfma_f32_16x16x32_bf16 v[64:67], a[64:67], v[44:47], v[64:67]\n"
"v_mfma_f32_16x16x32_bf16 v[68:71], a[68:71], v[44:47], v[68:71]\n"
"ds_read_b64_tr_b16 a[86:87], v6 offset:33800\n"
"s_waitcnt lgkmcnt(8)\n"
"v_mfma_f32_16x16x32_bf16 v[88:91], a[48:51], v[32:35], v[88:91]\n"
"ds_read_b128 v[44:47], v7 offset:23616\n"
"s_add_u32 m0, s62, 69632\n"
"v_mfma_f32_16x16x32_bf16 v[92:95], a[52:55], v[32:35], v[92:95]\n"
"buffer_load_dwordx4 v4, s[52:55], s78 offen lds\n"
"v_mfma_f32_16x16x32_bf16 v[80:83], a[56:59], v[32:35], v[80:83]\n"
"ds_read_b64_tr_b16 a[88:89], v6 offset:40960\n"
"v_mfma_f32_16x16x32_bf16 v[84:87], a[60:63], v[32:35], v[84:87]\n"
"v_mfma_f32_16x16x32_bf16 v[72:75], a[64:67], v[32:35], v[72:75]\n"
"s_add_u32 s77, s71, s76\n"
"s_add_u32 s78, s77, 4096\n"
"v_mfma_f32_16x16x32_bf16 v[76:79], a[68:71], v[32:35], v[76:79]\n"
"ds_read_b64_tr_b16 a[90:91], v6 offset:41984\n"
"s_waitcnt lgkmcnt(8)\n"
"v_mfma_f32_16x16x32_bf16 v[96:99], a[48:51], v[36:39], v[96:99]\n"
"ds_read_b128 v[32:35], v7 offset:16512\n"
"v_mfma_f32_16x16x32_bf16 v[100:103], a[52:55], v[36:39], v[100:103]\n"
"s_add_u32 m0, s63, 0\n"
"v_mfma_f32_16x16x32_bf16 v[88:91], a[56:59], v[36:39], v[88:91]\n"
"ds_read_b64_tr_b16 a[92:93], v6 offset:40968\n"
"v_mfma_f32_16x16x32_bf16 v[92:95], a[60:63], v[36:39], v[92:95]\n"
"buffer_load_dwordx4 v2, s[36:39], s77 offen nt lds\n"
"v_mfma_f32_16x16x32_bf16 v[80:83], a[64:67], v[36:39], v[80:83]\n"
"s_add_u32 m0, s63, 8256\n"
"v_mfma_f32_16x16x32_bf16 v[84:87], a[68:71], v[36:39], v[84:87]\n"
"ds_read_b64_tr_b16 a[94:95], v6 offset:41992\n"
"s_waitcnt lgkmcnt(8)\n"
"v_mfma_f32_16x16x32_bf16 v[104:107], a[48:51], v[40:43], v[104:107]\n"
"ds_read_b128 v[36:39], v7 offset:17536\n"
"v_mfma_f32_16x16x32_bf16 v[108:111], a[52:55], v[40:43], v[108:111]\n"
"buffer_load_dwordx4 v2, s[40:43], s77 offen nt lds\n"
"v_mfma_f32_16x16x32_bf16 v[96:99], a[56:59], v[40:43], v[96:99]\n"
"s_add_u32 m0, s63, 4096\n"
"v_mfma_f32_16x16x32_bf16 v[100:103], a[60:63], v[40:43], v[100:103]\n"
"buffer_load_dwordx4 v2, s[36:39], s78 offen nt lds\n"
"v_mfma_f32_16x16x32_bf16 v[88:91], a[64:67], v[40:43], v[88:91]\n"
"s_add_u32 m0, s63, 12352\n"
"v_mfma_f32_16x16x32_bf16 v[92:95], a[68:71], v[40:43], v[92:95]\n"
"s_waitcnt lgkmcnt(6)\n"
"v_mfma_f32_16x16x32_bf16 v[112:115], a[48:51], v[44:47], v[112:115]\n"
"ds_read_b128 v[40:43], v7 offset:18560\n"
"v_mfma_f32_16x16x32_bf16 v[116:119], a[52:55], v[44:47], v[116:119]\n"
"buffer_load_dwordx4 v2, s[40:43], s78 offen nt lds\n"
"v_mfma_f32_16x16x32_bf16 v[104:107], a[56:59], v[44:47], v[104:107]\n"
"v_mfma_f32_16x16x32_bf16 v[108:111], a[60:63], v[44:47], v[108:111]\n"
"v_mfma_f32_16x16x32_bf16 v[96:99], a[64:67], v[44:47], v[96:99]\n"
"v_mfma_f32_16x16x32_bf16 v[100:103], a[68:71], v[44:47], v[100:103]\n"
"s_waitcnt vmcnt(16) lgkmcnt(2)\n"
"s_barrier\n"
"v_mfma_f32_16x16x32_bf16 v[56:59], a[72:75], v[32:35], v[56:59]\n"
"ds_read_b128 v[44:47], v7 offset:19584\n"
"ds_read_b64_tr_b16 a[96:97], v5 offset:0\n"
"v_mfma_f32_16x16x32_bf16 v[60:63], a[76:79], v[32:35], v[60:63]\n"
"s_lshl_b32 s77, s71, 9\n"
"v_mfma_f32_16x16x32_bf16 v[48:51], a[80:83], v[32:35], v[48:51]\n"
"s_add_u32 s77, s77, 32768\n"
"v_mfma_f32_16x16x32_bf16 v[52:55], a[84:87], v[32:35], v[52:55]\n"
"ds_read_b64_tr_b16 a[98:99], v5 offset:1024\n"
"s_waitcnt lgkmcnt(4)\n"
"v_mfma_f32_16x16x32_bf16 v[64:67], a[72:75], v[36:39], v[64:67]\n"
"ds_read_b128 v[32:35], v7 offset:20608\n"
"v_mfma_f32_16x16x32_bf16 v[68:71], a[76:79], v[36:39], v[68:71]\n"
"s_add_u32 s78, s77, 16384\n"
"v_mfma_f32_16x16x32_bf16 v[56:59], a[80:83], v[36:39], v[56:59]\n"
"ds_read_b64_tr_b16 a[100:101], v5 offset:8\n"
"v_mfma_f32_16x16x32_bf16 v[60:63], a[84:87], v[36:39], v[60:63]\n"
"s_add_u32 m0, s62, 73728\n"
"v_mfma_f32_16x16x32_bf16 v[48:51], a[88:91], v[36:39], v[48:51]\n"
"buffer_load_dwordx4 v4, s[44:47], s77 offen lds\n"
"v_mfma_f32_16x16x32_bf16 v[52:55], a[92:95], v[36:39], v[52:55]\n"
"ds_read_b64_tr_b16 a[102:103], v5 offset:1032\n"
"s_waitcnt lgkmcnt(6)\n"
"v_mfma_f32_16x16x32_bf16 v[72:75], a[72:75], v[40:43], v[72:75]\n"
"ds_read_b128 v[36:39], v7 offset:21632\n"
"v_mfma_f32_16x16x32_bf16 v[76:79], a[76:79], v[40:43], v[76:79]\n"
"v_mfma_f32_16x16x32_bf16 v[64:67], a[80:83], v[40:43], v[64:67]\n"
"ds_read_b64_tr_b16 a[104:105], v5 offset:8192\n"
"v_mfma_f32_16x16x32_bf16 v[68:71], a[84:87], v[40:43], v[68:71]\n"
"s_add_u32 m0, s62, 77824\n"
"v_mfma_f32_16x16x32_bf16 v[56:59], a[88:91], v[40:43], v[56:59]\n"
"buffer_load_dwordx4 v4, s[44:47], s78 offen lds\n"
"v_mfma_f32_16x16x32_bf16 v[60:63], a[92:95], v[40:43], v[60:63]\n"
"ds_read_b64_tr_b16 a[106:107], v5 offset:9216\n"
"s_waitcnt lgkmcnt(8)\n"
"v_mfma_f32_16x16x32_bf16 v[80:83], a[72:75], v[44:47], v[80:83]\n"
"ds_read_b128 v[40:43], v7 offset:22656\n"
"v_mfma_f32_16x16x32_bf16 v[84:87], a[76:79], v[44:47], v[84:87]\n"
"v_mfma_f32_16x16x32_bf16 v[72:75], a[80:83], v[44:47], v[72:75]\n"
"ds_read_b64_tr_b16 a[108:109], v5 offset:8200\n"
"v_mfma_f32_16x16x32_bf16 v[76:79], a[84:87], v[44:47], v[76:79]\n"
"s_add_u32 m0, s62, 81920\n"
"v_mfma_f32_16x16x32_bf16 v[64:67], a[88:91], v[44:47], v[64:67]\n"
"buffer_load_dwordx4 v4, s[48:51], s77 offen lds\n"
"v_mfma_f32_16x16x32_bf16 v[68:71], a[92:95], v[44:47], v[68:71]\n"
"ds_read_b64_tr_b16 a[110:111], v5 offset:9224\n"
"s_waitcnt lgkmcnt(8)\n"
"v_mfma_f32_16x16x32_bf16 v[88:91], a[72:75], v[32:35], v[88:91]\n"
"ds_read_b128 v[44:47], v7 offset:23680\n"
"v_mfma_f32_16x16x32_bf16 v[92:95], a[76:79], v[32:35], v[92:95]\n"
"v_mfma_f32_16x16x32_bf16 v[80:83], a[80:83], v[32:35], v[80:83]\n"
"ds_read_b64_tr_b16 a[112:113], v5 offset:16384\n"
"v_mfma_f32_16x16x32_bf16 v[84:87], a[84:87], v[32:35], v[84:87]\n"
"s_add_u32 m0, s62, 86016\n"
"v_mfma_f32_16x16x32_bf16 v[72:75], a[88:91], v[32:35], v[72:75]\n"
"buffer_load_dwordx4 v4, s[48:51], s78 offen lds\n"
"v_mfma_f32_16x16x32_bf16 v[76:79], a[92:95], v[32:35], v[76:79]\n"
"ds_read_b64_tr_b16 a[114:115], v5 offset:17408\n"
"s_waitcnt lgkmcnt(8)\n"
"v_mfma_f32_16x16x32_bf16 v[96:99], a[72:75], v[36:39], v[96:99]\n"
"ds_read_b128 v[32:35], v7 offset:32896\n"
"v_mfma_f32_16x16x32_bf16 v[100:103], a[76:79], v[36:39], v[100:103]\n"
"v_mfma_f32_16x16x32_bf16 v[88:91], a[80:83], v[36:39], v[88:91]\n"
"ds_read_b64_tr_b16 a[116:117], v5 offset:16392\n"
"v_mfma_f32_16x16x32_bf16 v[92:95], a[84:87], v[36:39], v[92:95]\n"
"s_add_u32 m0, s62, 90112\n"
"v_mfma_f32_16x16x32_bf16 v[80:83], a[88:91], v[36:39], v[80:83]\n"
"v_mfma_f32_16x16x32_bf16 v[84:87], a[92:95], v[36:39], v[84:87]\n"
"ds_read_b64_tr_b16 a[118:119], v5 offset:17416\n"
"s_waitcnt lgkmcnt(8)\n"
"v_mfma_f32_16x16x32_bf16 v[104:107], a[72:75], v[40:43], v[104:107]\n"
"ds_read_b128 v[36:39], v7 offset:33920\n"
"v_mfma_f32_16x16x32_bf16 v[108:111], a[76:79], v[40:43], v[108:111]\n"
"buffer_load_dwordx4 v4, s[52:55], s77 offen lds\n"
"v_mfma_f32_16x16x32_bf16 v[96:99], a[80:83], v[40:43], v[96:99]\n"
"v_mfma_f32_16x16x32_bf16 v[100:103], a[84:87], v[40:43], v[100:103]\n"
"s_add_u32 m0, s62, 94208\n"
"v_mfma_f32_16x16x32_bf16 v[88:91], a[88:91], v[40:43], v[88:91]\n"
"buffer_load_dwordx4 v4, s[52:55], s78 offen lds\n"
"v_mfma_f32_16x16x32_bf16 v[92:95], a[92:95], v[40:43], v[92:95]\n"
"s_waitcnt lgkmcnt(6)\n"
"v_mfma_f32_16x16x32_bf16 v[112:115], a[72:75], v[44:47], v[112:115]\n"
"ds_read_b128 v[40:43], v7 offset:34944\n"
"v_mfma_f32_16x16x32_bf16 v[116:119], a[76:79], v[44:47], v[116:119]\n"
"v_mfma_f32_16x16x32_bf16 v[104:107], a[80:83], v[44:47], v[104:107]\n"
"v_mfma_f32_16x16x32_bf16 v[108:111], a[84:87], v[44:47], v[108:111]\n"
"v_mfma_f32_16x16x32_bf16 v[96:99], a[88:91], v[44:47], v[96:99]\n"
"v_mfma_f32_16x16x32_bf16 v[100:103], a[92:95], v[44:47], v[100:103]\n"
"s_waitcnt vmcnt(16) lgkmcnt(2)\n"
"s_barrier\n"
"v_mfma_f32_16x16x32_bf16 v[56:59], a[96:99], v[32:35], v[56:59]\n"
"ds_read_b128 v[44:47], v7 offset:35968\n"
"ds_read_b64_tr_b16 a[120:121], v5 offset:24576\n"
"v_mfma_f32_16x16x32_bf16 v[60:63], a[100:103], v[32:35], v[60:63]\n"
"s_lshl_b32 s77, s72, 9\n"
"s_add_u32 s78, s77, 16384\n"
"v_mfma_f32_16x16x32_bf16 v[48:51], a[104:107], v[32:35], v[48:51]\n"
"s_add_u32 m0, s62, 0\n"
"v_mfma_f32_16x16x32_bf16 v[52:55], a[108:111], v[32:35], v[52:55]\n"
"ds_read_b64_tr_b16 a[122:123], v5 offset:25600\n"
"s_waitcnt lgkmcnt(4)\n"
"v_mfma_f32_16x16x32_bf16 v[64:67], a[96:99], v[36:39], v[64:67]\n"
"ds_read_b128 v[32:35], v7 offset:36992\n"
"v_mfma_f32_16x16x32_bf16 v[68:71], a[100:103], v[36:39], v[68:71]\n"
"buffer_load_dwordx4 v4, s[44:47], s77 offen lds\n"
"v_mfma_f32_16x16x32_bf16 v[56:59], a[104:107], v[36:39], v[56:59]\n"
"ds_read_b64_tr_b16 a[124:125], v5 offset:24584\n"
"v_mfma_f32_16x16x32_bf16 v[60:63], a[108:111], v[36:39], v[60:63]\n"
"s_add_u32 m0, s62, 4096\n"
"v_mfma_f32_16x16x32_bf16 v[48:51], a[112:115], v[36:39], v[48:51]\n"
"buffer_load_dwordx4 v4, s[44:47], s78 offen lds\n"
"v_mfma_f32_16x16x32_bf16 v[52:55], a[116:119], v[36:39], v[52:55]\n"
"ds_read_b64_tr_b16 a[126:127], v5 offset:25608\n"
"s_waitcnt lgkmcnt(6)\n"
"v_mfma_f32_16x16x32_bf16 v[72:75], a[96:99], v[40:43], v[72:75]\n"
"ds_read_b128 v[36:39], v7 offset:38016\n"
"v_mfma_f32_16x16x32_bf16 v[76:79], a[100:103], v[40:43], v[76:79]\n"
"s_add_u32 m0, s62, 8192\n"
"v_mfma_f32_16x16x32_bf16 v[64:67], a[104:107], v[40:43], v[64:67]\n"
"ds_read_b64_tr_b16 a[128:129], v5 offset:32768\n"
"v_mfma_f32_16x16x32_bf16 v[68:71], a[108:111], v[40:43], v[68:71]\n"
"buffer_load_dwordx4 v4, s[48:51], s77 offen lds\n"
"v_mfma_f32_16x16x32_bf16 v[56:59], a[112:115], v[40:43], v[56:59]\n"
"s_add_u32 m0, s62, 12288\n"
"v_mfma_f32_16x16x32_bf16 v[60:63], a[116:119], v[40:43], v[60:63]\n"
"ds_read_b64_tr_b16 a[130:131], v5 offset:33792\n"
"s_waitcnt lgkmcnt(8)\n"
"v_mfma_f32_16x16x32_bf16 v[80:83], a[96:99], v[44:47], v[80:83]\n"
"ds_read_b128 v[40:43], v7 offset:39040\n"
"v_mfma_f32_16x16x32_bf16 v[84:87], a[100:103], v[44:47], v[84:87]\n"
"buffer_load_dwordx4 v4, s[48:51], s78 offen lds\n"
"v_mfma_f32_16x16x32_bf16 v[72:75], a[104:107], v[44:47], v[72:75]\n"
"ds_read_b64_tr_b16 a[132:133], v5 offset:32776\n"
"s_add_u32 m0, s62, 16384\n"
"v_mfma_f32_16x16x32_bf16 v[76:79], a[108:111], v[44:47], v[76:79]\n"
"buffer_load_dwordx4 v4, s[52:55], s77 offen lds\n"
"v_mfma_f32_16x16x32_bf16 v[64:67], a[112:115], v[44:47], v[64:67]\n"
"v_mfma_f32_16x16x32_bf16 v[68:71], a[116:119], v[44:47], v[68:71]\n"
"ds_read_b64_tr_b16 a[134:135], v5 offset:33800\n"
"s_waitcnt lgkmcnt(8)\n"
"v_mfma_f32_16x16x32_bf16 v[88:91], a[96:99], v[32:35], v[88:91]\n"
"ds_read_b128 v[44:47], v7 offset:40064\n"
"s_add_u32 m0, s62, 20480\n"
"v_mfma_f32_16x16x32_bf16 v[92:95], a[100:103], v[32:35], v[92:95]\n"
"buffer_load_dwordx4 v4, s[52:55], s78 offen lds\n"
"v_mfma_f32_16x16x32_bf16 v[80:83], a[104:107], v[32:35], v[80:83]\n"
"ds_read_b64_tr_b16 a[136:137], v5 offset:40960\n"
"v_mfma_f32_16x16x32_bf16 v[84:87], a[108:111], v[32:35], v[84:87]\n"
"v_mfma_f32_16x16x32_bf16 v[72:75], a[112:115], v[32:35], v[72:75]\n"
"s_add_u32 s77, s72, s76\n"
"s_add_u32 s78, s77, 4096\n"
"v_mfma_f32_16x16x32_bf16 v[76:79], a[116:119], v[32:35], v[76:79]\n"
"ds_read_b64_tr_b16 a[138:139], v5 offset:41984\n"
"s_waitcnt lgkmcnt(8)\n"
"v_mfma_f32_16x16x32_bf16 v[96:99], a[96:99], v[36:39], v[96:99]\n"
"ds_read_b128 v[32:35], v7 offset:32960\n"
"v_mfma_f32_16x16x32_bf16 v[100:103], a[100:103], v[36:39], v[100:103]\n"
"s_add_u32 m0, s63, 16448\n"
"v_mfma_f32_16x16x32_bf16 v[88:91], a[104:107], v[36:39], v[88:91]\n"
"ds_read_b64_tr_b16 a[140:141], v5 offset:40968\n"
"v_mfma_f32_16x16x32_bf16 v[92:95], a[108:111], v[36:39], v[92:95]\n"
"buffer_load_dwordx4 v2, s[36:39], s77 offen nt lds\n"
"v_mfma_f32_16x16x32_bf16 v[80:83], a[112:115], v[36:39], v[80:83]\n"
"s_add_u32 m0, s63, 24704\n"
"v_mfma_f32_16x16x32_bf16 v[84:87], a[116:119], v[36:39], v[84:87]\n"
"ds_read_b64_tr_b16 a[142:143], v5 offset:41992\n"
"s_waitcnt lgkmcnt(8)\n"
"v_mfma_f32_16x16x32_bf16 v[104:107], a[96:99], v[40:43], v[104:107]\n"
"ds_read_b128 v[36:39], v7 offset:33984\n"
"v_mfma_f32_16x16x32_bf16 v[108:111], a[100:103], v[40:43], v[108:111]\n"
"buffer_load_dwordx4 v2, s[40:43], s77 offen nt lds\n"
"v_mfma_f32_16x16x32_bf16 v[96:99], a[104:107], v[40:43], v[96:99]\n"
"s_add_u32 m0, s63, 20544\n"
"v_mfma_f32_16x16x32_bf16 v[100:103], a[108:111], v[40:43], v[100:103]\n"
"buffer_load_dwordx4 v2, s[36:39], s78 offen nt lds\n"
"v_mfma_f32_16x16x32_bf16 v[88:91], a[112:115], v[40:43], v[88:91]\n"
"s_add_u32 m0, s63, 28800\n"
"v_mfma_f32_16x16x32_bf16 v[92:95], a[116:119], v[40:43], v[92:95]\n"
"s_waitcnt lgkmcnt(6)\n"
"v_mfma_f32_16x16x32_bf16 v[112:115], a[96:99], v[44:47], v[112:115]\n"
"ds_read_b128 v[40:43], v7 offset:35008\n"
"v_mfma_f32_16x16x32_bf16 v[116:119], a[100:103], v[44:47], v[116:119]\n"
"buffer_load_dwordx4 v2, s[40:43], s78 offen nt lds\n"
"v_mfma_f32_16x16x32_bf16 v[104:107], a[104:107], v[44:47], v[104:107]\n"
"v_mfma_f32_16x16x32_bf16 v[108:111], a[108:111], v[44:47], v[108:111]\n"
"v_mfma_f32_16x16x32_bf16 v[96:99], a[112:115], v[44:47], v[96:99]\n"
"v_mfma_f32_16x16x32_bf16 v[100:103], a[116:119], v[44:47], v[100:103]\n"
"s_waitcnt vmcnt(16) lgkmcnt(2)\n"
"s_barrier\n"
"v_mfma_f32_16x16x32_bf16 v[56:59], a[120:123], v[32:35], v[56:59]\n"
"ds_read_b128 v[44:47], v7 offset:36032\n"
"ds_read_b64_tr_b16 a[144:145], v6 offset:0\n"
"v_mfma_f32_16x16x32_bf16 v[60:63], a[124:127], v[32:35], v[60:63]\n"
"s_lshl_b32 s77, s72, 9\n"
"v_lshlrev_b32 v8, 16, v236\n"
"v_mfma_f32_16x16x32_bf16 v[48:51], a[128:131], v[32:35], v[48:51]\n"
"s_add_u32 s77, s77, 32768\n"
"v_add_f32 v8, 0x3727c5ac, v8\n"
"v_mfma_f32_16x16x32_bf16 v[52:55], a[132:135], v[32:35], v[52:55]\n"
"ds_read_b64_tr_b16 a[146:147], v6 offset:1024\n"
"s_waitcnt lgkmcnt(4)\n"
"v_mfma_f32_16x16x32_bf16 v[64:67], a[120:123], v[36:39], v[64:67]\n"
"ds_read_b128 v[32:35], v7 offset:37056\n"
"v_mfma_f32_16x16x32_bf16 v[68:71], a[124:127], v[36:39], v[68:71]\n"
"s_add_u32 s78, s77, 16384\n"
"v_mfma_f32_16x16x32_bf16 v[56:59], a[128:131], v[36:39], v[56:59]\n"
"ds_read_b64_tr_b16 a[148:149], v6 offset:8\n"
"v_mfma_f32_16x16x32_bf16 v[60:63], a[132:135], v[36:39], v[60:63]\n"
"s_add_u32 m0, s62, 24576\n"
"v_mfma_f32_16x16x32_bf16 v[48:51], a[136:139], v[36:39], v[48:51]\n"
"buffer_load_dwordx4 v4, s[44:47], s77 offen lds\n"
"v_mfma_f32_16x16x32_bf16 v[52:55], a[140:143], v[36:39], v[52:55]\n"
"ds_read_b64_tr_b16 a[150:151], v6 offset:1032\n"
"s_waitcnt lgkmcnt(6)\n"
"v_mfma_f32_16x16x32_bf16 v[72:75], a[120:123], v[40:43], v[72:75]\n"
"ds_read_b128 v[36:39], v7 offset:38080\n"
"v_mfma_f32_16x16x32_bf16 v[76:79], a[124:127], v[40:43], v[76:79]\n"
"v_rsq_f32 v8, v8\n"
"v_mfma_f32_16x16x32_bf16 v[64:67], a[128:131], v[40:43], v[64:67]\n"
"ds_read_b64_tr_b16 a[152:153], v6 offset:8192\n"
"v_lshlrev_b32 v249, 16, v240\n"
"v_mfma_f32_16x16x32_bf16 v[68:71], a[132:135], v[40:43], v[68:71]\n"
"s_add_u32 m0, s62, 28672\n"
"v_mul_f32 v8, v8, v249\n"
"v_mfma_f32_16x16x32_bf16 v[56:59], a[136:139], v[40:43], v[56:59]\n"
"buffer_load_dwordx4 v4, s[44:47], s78 offen lds\n"
"v_mfma_f32_16x16x32_bf16 v[60:63], a[140:143], v[40:43], v[60:63]\n"
"ds_read_b64_tr_b16 a[154:155], v6 offset:9216\n"
"s_waitcnt lgkmcnt(8)\n"
"v_mfma_f32_16x16x32_bf16 v[80:83], a[120:123], v[44:47], v[80:83]\n"
"ds_read_b128 v[40:43], v7 offset:39104\n"
"v_lshlrev_b32 v249, 16, v232\n"
"v_mfma_f32_16x16x32_bf16 v[84:87], a[124:127], v[44:47], v[84:87]\n"
"v_lshlrev_b32 v16, 16, v244\n"
"v_mfma_f32_16x16x32_bf16 v[72:75], a[128:131], v[44:47], v[72:75]\n"
"ds_read_b64_tr_b16 a[156:157], v6 offset:8200\n"
"v_fma_f32 v16, -v249, v8, v16\n"
"v_mfma_f32_16x16x32_bf16 v[76:79], a[132:135], v[44:47], v[76:79]\n"
"s_add_u32 m0, s62, 32768\n"
"v_mul_f32 v8, s83, v8\n"
"v_mfma_f32_16x16x32_bf16 v[64:67], a[136:139], v[44:47], v[64:67]\n"
"buffer_load_dwordx4 v4, s[48:51], s77 offen lds\n"
"v_mfma_f32_16x16x32_bf16 v[68:71], a[140:143], v[44:47], v[68:71]\n"
"ds_read_b64_tr_b16 a[158:159], v6 offset:9224\n"
"s_waitcnt lgkmcnt(8)\n"
"v_mfma_f32_16x16x32_bf16 v[88:91], a[120:123], v[32:35], v[88:91]\n"
"ds_read_b128 v[44:47], v7 offset:40128\n"
"v_mfma_f32_16x16x32_bf16 v[92:95], a[124:127], v[32:35], v[92:95]\n"
"v_mul_f32 v16, s83, v16\n"
"v_mfma_f32_16x16x32_bf16 v[80:83], a[128:131], v[32:35], v[80:83]\n"
"ds_read_b64_tr_b16 a[160:161], v6 offset:16384\n"
"v_and_b32 v9, 0xffff0000, v236\n"
"v_mfma_f32_16x16x32_bf16 v[84:87], a[132:135], v[32:35], v[84:87]\n"
"s_add_u32 m0, s62, 36864\n"
"v_mfma_f32_16x16x32_bf16 v[72:75], a[136:139], v[32:35], v[72:75]\n"
"buffer_load_dwordx4 v4, s[48:51], s78 offen lds\n"
"v_mfma_f32_16x16x32_bf16 v[76:79], a[140:143], v[32:35], v[76:79]\n"
"ds_read_b64_tr_b16 a[162:163], v6 offset:17408\n"
"s_waitcnt lgkmcnt(8)\n"
"v_mfma_f32_16x16x32_bf16 v[96:99], a[120:123], v[36:39], v[96:99]\n"
"ds_read_b128 v[32:35], v7 offset:0\n"
"v_add_f32 v9, 0x3727c5ac, v9\n"
"v_mfma_f32_16x16x32_bf16 v[100:103], a[124:127], v[36:39], v[100:103]\n"
"v_rsq_f32 v9, v9\n"
"v_mfma_f32_16x16x32_bf16 v[88:91], a[128:131], v[36:39], v[88:91]\n"
"ds_read_b64_tr_b16 a[164:165], v6 offset:16392\n"
"v_mfma_f32_16x16x32_bf16 v[92:95], a[132:135], v[36:39], v[92:95]\n"
"s_add_u32 m0, s62, 40960\n"
"v_mfma_f32_16x16x32_bf16 v[80:83], a[136:139], v[36:39], v[80:83]\n"
"v_and_b32 v249, 0xffff0000, v240\n"
"v_mfma_f32_16x16x32_bf16 v[84:87], a[140:143], v[36:39], v[84:87]\n"
"ds_read_b64_tr_b16 a[166:167], v6 offset:17416\n"
"s_waitcnt lgkmcnt(8)\n"
"v_mfma_f32_16x16x32_bf16 v[104:107], a[120:123], v[40:43], v[104:107]\n"
"ds_read_b128 v[36:39], v7 offset:1024\n"
"v_mul_f32 v9, v9, v249\n"
"v_mfma_f32_16x16x32_bf16 v[108:111], a[124:127], v[40:43], v[108:111]\n"
"buffer_load_dwordx4 v4, s[52:55], s77 offen lds\n"
"v_mfma_f32_16x16x32_bf16 v[96:99], a[128:131], v[40:43], v[96:99]\n"
"v_and_b32 v249, 0xffff0000, v232\n"
"v_mfma_f32_16x16x32_bf16 v[100:103], a[132:135], v[40:43], v[100:103]\n"
"s_add_u32 m0, s62, 45056\n"
"v_and_b32 v17, 0xffff0000, v244\n"
"v_mfma_f32_16x16x32_bf16 v[88:91], a[136:139], v[40:43], v[88:91]\n"
"buffer_load_dwordx4 v4, s[52:55], s78 offen lds\n"
"v_mfma_f32_16x16x32_bf16 v[92:95], a[140:143], v[40:43], v[92:95]\n"
"s_waitcnt lgkmcnt(6)\n"
"v_mfma_f32_16x16x32_bf16 v[112:115], a[120:123], v[44:47], v[112:115]\n"
"ds_read_b128 v[40:43], v7 offset:2048\n"
"v_fma_f32 v17, -v249, v9, v17\n"
"v_mfma_f32_16x16x32_bf16 v[116:119], a[124:127], v[44:47], v[116:119]\n"
"v_mul_f32 v9, s83, v9\n"
"v_mfma_f32_16x16x32_bf16 v[104:107], a[128:131], v[44:47], v[104:107]\n"
"v_mul_f32 v17, s83, v17\n"
"v_mfma_f32_16x16x32_bf16 v[108:111], a[132:135], v[44:47], v[108:111]\n"
"v_mfma_f32_16x16x32_bf16 v[96:99], a[136:139], v[44:47], v[96:99]\n"
"v_mfma_f32_16x16x32_bf16 v[100:103], a[140:143], v[44:47], v[100:103]\n"
"s_waitcnt vmcnt(16) lgkmcnt(2)\n"
"s_barrier\n"
"v_mfma_f32_16x16x32_bf16 v[56:59], a[144:147], v[32:35], v[56:59]\n"
"ds_read_b128 v[44:47], v7 offset:3072\n"
"ds_read_b64_tr_b16 a[168:169], v6 offset:24576\n"
"v_mfma_f32_16x16x32_bf16 v[60:63], a[148:151], v[32:35], v[60:63]\n"
"s_lshl_b32 s77, s73, 9\n"
"s_add_u32 s78, s77, 16384\n"
"v_mfma_f32_16x16x32_bf16 v[48:51], a[152:155], v[32:35], v[48:51]\n"
"s_add_u32 m0, s62, 49152\n"
"v_mfma_f32_16x16x32_bf16 v[52:55], a[156:159], v[32:35], v[52:55]\n"
"ds_read_b64_tr_b16 a[170:171], v6 offset:25600\n"
"s_waitcnt lgkmcnt(4)\n"
"v_mfma_f32_16x16x32_bf16 v[64:67], a[144:147], v[36:39], v[64:67]\n"
"ds_read_b128 v[32:35], v7 offset:4096\n"
"v_mfma_f32_16x16x32_bf16 v[68:71], a[148:151], v[36:39], v[68:71]\n"
"buffer_load_dwordx4 v4, s[44:47], s77 offen lds\n"
"v_mfma_f32_16x16x32_bf16 v[56:59], a[152:155], v[36:39], v[56:59]\n"
"ds_read_b64_tr_b16 a[172:173], v6 offset:24584\n"
"v_mfma_f32_16x16x32_bf16 v[60:63], a[156:159], v[36:39], v[60:63]\n"
"s_add_u32 m0, s62, 53248\n"
"v_mfma_f32_16x16x32_bf16 v[48:51], a[160:163], v[36:39], v[48:51]\n"
"buffer_load_dwordx4 v4, s[44:47], s78 offen lds\n"
"v_mfma_f32_16x16x32_bf16 v[52:55], a[164:167], v[36:39], v[52:55]\n"
"ds_read_b64_tr_b16 a[174:175], v6 offset:25608\n"
"s_waitcnt lgkmcnt(6)\n"
"v_mfma_f32_16x16x32_bf16 v[72:75], a[144:147], v[40:43], v[72:75]\n"
"ds_read_b128 v[36:39], v7 offset:5120\n"
"v_mfma_f32_16x16x32_bf16 v[76:79], a[148:151], v[40:43], v[76:79]\n"
"s_add_u32 m0, s62, 57344\n"
"v_mfma_f32_16x16x32_bf16 v[64:67], a[152:155], v[40:43], v[64:67]\n"
"ds_read_b64_tr_b16 a[176:177], v6 offset:32768\n"
"v_mfma_f32_16x16x32_bf16 v[68:71], a[156:159], v[40:43], v[68:71]\n"
"buffer_load_dwordx4 v4, s[48:51], s77 offen lds\n"
"v_mfma_f32_16x16x32_bf16 v[56:59], a[160:163], v[40:43], v[56:59]\n"
"s_add_u32 m0, s62, 61440\n"
"v_mfma_f32_16x16x32_bf16 v[60:63], a[164:167], v[40:43], v[60:63]\n"
"ds_read_b64_tr_b16 a[178:179], v6 offset:33792\n"
"s_waitcnt lgkmcnt(8)\n"
"v_mfma_f32_16x16x32_bf16 v[80:83], a[144:147], v[44:47], v[80:83]\n"
"ds_read_b128 v[40:43], v7 offset:6144\n"
"v_mfma_f32_16x16x32_bf16 v[84:87], a[148:151], v[44:47], v[84:87]\n"
"buffer_load_dwordx4 v4, s[48:51], s78 offen lds\n"
"v_mfma_f32_16x16x32_bf16 v[72:75], a[152:155], v[44:47], v[72:75]\n"
"ds_read_b64_tr_b16 a[180:181], v6 offset:32776\n"
"s_add_u32 m0, s62, 65536\n"
"v_mfma_f32_16x16x32_bf16 v[76:79], a[156:159], v[44:47], v[76:79]\n"
"buffer_load_dwordx4 v4, s[52:55], s77 offen lds\n"
"v_mfma_f32_16x16x32_bf16 v[64:67], a[160:163], v[44:47], v[64:67]\n"
"v_mfma_f32_16x16x32_bf16 v[68:71], a[164:167], v[44:47], v[68:71]\n"
"ds_read_b64_tr_b16 a[182:183], v6 offset:33800\n"
"s_waitcnt lgkmcnt(8)\n"
"v_mfma_f32_16x16x32_bf16 v[88:91], a[144:147], v[32:35], v[88:91]\n"
"ds_read_b128 v[44:47], v7 offset:7168\n"
"s_add_u32 m0, s62, 69632\n"
"v_mfma_f32_16x16x32_bf16 v[92:95], a[148:151], v[32:35], v[92:95]\n"
"buffer_load_dwordx4 v4, s[52:55], s78 offen lds\n"
"v_mfma_f32_16x16x32_bf16 v[80:83], a[152:155], v[32:35], v[80:83]\n"
"ds_read_b64_tr_b16 a[184:185], v6 offset:40960\n"
"v_mfma_f32_16x16x32_bf16 v[84:87], a[156:159], v[32:35], v[84:87]\n"
"v_mfma_f32_16x16x32_bf16 v[72:75], a[160:163], v[32:35], v[72:75]\n"
"s_add_u32 s77, s73, s76\n"
"s_add_u32 s78, s77, 4096\n"
"v_mfma_f32_16x16x32_bf16 v[76:79], a[164:167], v[32:35], v[76:79]\n"
"ds_read_b64_tr_b16 a[186:187], v6 offset:41984\n"
"s_waitcnt lgkmcnt(8)\n"
"v_mfma_f32_16x16x32_bf16 v[96:99], a[144:147], v[36:39], v[96:99]\n"
"ds_read_b128 v[32:35], v7 offset:64\n"
"v_mfma_f32_16x16x32_bf16 v[100:103], a[148:151], v[36:39], v[100:103]\n"
"s_add_u32 m0, s63, 32896\n"
"v_mfma_f32_16x16x32_bf16 v[88:91], a[152:155], v[36:39], v[88:91]\n"
"ds_read_b64_tr_b16 a[188:189], v6 offset:40968\n"
"v_mfma_f32_16x16x32_bf16 v[92:95], a[156:159], v[36:39], v[92:95]\n"
"buffer_load_dwordx4 v2, s[36:39], s77 offen nt lds\n"
"v_mfma_f32_16x16x32_bf16 v[80:83], a[160:163], v[36:39], v[80:83]\n"
"s_add_u32 m0, s63, 41152\n"
"v_mfma_f32_16x16x32_bf16 v[84:87], a[164:167], v[36:39], v[84:87]\n"
"ds_read_b64_tr_b16 a[190:191], v6 offset:41992\n"
"s_waitcnt lgkmcnt(8)\n"
"v_mfma_f32_16x16x32_bf16 v[104:107], a[144:147], v[40:43], v[104:107]\n"
"ds_read_b128 v[36:39], v7 offset:1088\n"
"v_mfma_f32_16x16x32_bf16 v[108:111], a[148:151], v[40:43], v[108:111]\n"
"buffer_load_dwordx4 v2, s[40:43], s77 offen nt lds\n"
"v_mfma_f32_16x16x32_bf16 v[96:99], a[152:155], v[40:43], v[96:99]\n"
"s_add_u32 m0, s63, 36992\n"
"v_mfma_f32_16x16x32_bf16 v[100:103], a[156:159], v[40:43], v[100:103]\n"
"buffer_load_dwordx4 v2, s[36:39], s78 offen nt lds\n"
"v_mfma_f32_16x16x32_bf16 v[88:91], a[160:163], v[40:43], v[88:91]\n"
"s_add_u32 m0, s63, 45248\n"
"v_mfma_f32_16x16x32_bf16 v[92:95], a[164:167], v[40:43], v[92:95]\n"
"s_waitcnt lgkmcnt(6)\n"
"v_mfma_f32_16x16x32_bf16 v[112:115], a[144:147], v[44:47], v[112:115]\n"
"ds_read_b128 v[40:43], v7 offset:2112\n"
"v_mfma_f32_16x16x32_bf16 v[116:119], a[148:151], v[44:47], v[116:119]\n"
"buffer_load_dwordx4 v2, s[40:43], s78 offen nt lds\n"
"v_mfma_f32_16x16x32_bf16 v[104:107], a[152:155], v[44:47], v[104:107]\n"
"v_mfma_f32_16x16x32_bf16 v[108:111], a[156:159], v[44:47], v[108:111]\n"
"v_mfma_f32_16x16x32_bf16 v[96:99], a[160:163], v[44:47], v[96:99]\n"
"v_mfma_f32_16x16x32_bf16 v[100:103], a[164:167], v[44:47], v[100:103]\n"
"s_waitcnt vmcnt(16) lgkmcnt(2)\n"
"s_barrier\n"
"v_mfma_f32_16x16x32_bf16 v[56:59], a[168:171], v[32:35], v[56:59]\n"
"ds_read_b128 v[44:47], v7 offset:3136\n"
"ds_read_b64_tr_b16 a[192:193], v5 offset:0\n"
"v_mfma_f32_16x16x32_bf16 v[60:63], a[172:175], v[32:35], v[60:63]\n"
"s_lshl_b32 s77, s73, 9\n"
"v_lshlrev_b32 v10, 16, v237\n"
"v_mfma_f32_16x16x32_bf16 v[48:51], a[176:179], v[32:35], v[48:51]\n"
"s_add_u32 s77, s77, 32768\n"
"v_add_f32 v10, 0x3727c5ac, v10\n"
"v_mfma_f32_16x16x32_bf16 v[52:55], a[180:183], v[32:35], v[52:55]\n"
"ds_read_b64_tr_b16 a[194:195], v5 offset:1024\n"
"s_waitcnt lgkmcnt(4)\n"
"v_mfma_f32_16x16x32_bf16 v[64:67], a[168:171], v[36:39], v[64:67]\n"
"ds_read_b128 v[32:35], v7 offset:4160\n"
"v_mfma_f32_16x16x32_bf16 v[68:71], a[172:175], v[36:39], v[68:71]\n"
"s_add_u32 s78, s77, 16384\n"
"v_mfma_f32_16x16x32_bf16 v[56:59], a[176:179], v[36:39], v[56:59]\n"
"ds_read_b64_tr_b16 a[196:197], v5 offset:8\n"
"v_mfma_f32_16x16x32_bf16 v[60:63], a[180:183], v[36:39], v[60:63]\n"
"s_add_u32 m0, s62, 73728\n"
"v_mfma_f32_16x16x32_bf16 v[48:51], a[184:187], v[36:39], v[48:51]\n"
"buffer_load_dwordx4 v4, s[44:47], s77 offen lds\n"
"v_mfma_f32_16x16x32_bf16 v[52:55], a[188:191], v[36:39], v[52:55]\n"
"ds_read_b64_tr_b16 a[198:199], v5 offset:1032\n"
"s_waitcnt lgkmcnt(6)\n"
"v_mfma_f32_16x16x32_bf16 v[72:75], a[168:171], v[40:43], v[72:75]\n"
"ds_read_b128 v[36:39], v7 offset:5184\n"
"v_mfma_f32_16x16x32_bf16 v[76:79], a[172:175], v[40:43], v[76:79]\n"
"v_rsq_f32 v10, v10\n"
"v_mfma_f32_16x16x32_bf16 v[64:67], a[176:179], v[40:43], v[64:67]\n"
"ds_read_b64_tr_b16 a[200:201], v5 offset:8192\n"
"v_lshlrev_b32 v249, 16, v241\n"
"v_mfma_f32_16x16x32_bf16 v[68:71], a[180:183], v[40:43], v[68:71]\n"
"s_add_u32 m0, s62, 77824\n"
"v_mul_f32 v10, v10, v249\n"
"v_mfma_f32_16x16x32_bf16 v[56:59], a[184:187], v[40:43], v[56:59]\n"
"buffer_load_dwordx4 v4, s[44:47], s78 offen lds\n"
"v_mfma_f32_16x16x32_bf16 v[60:63], a[188:191], v[40:43], v[60:63]\n"
"ds_read_b64_tr_b16 a[202:203], v5 offset:9216\n"
"s_waitcnt lgkmcnt(8)\n"
"v_mfma_f32_16x16x32_bf16 v[80:83], a[168:171], v[44:47], v[80:83]\n"
"ds_read_b128 v[40:43], v7 offset:6208\n"
"v_lshlrev_b32 v249, 16, v233\n"
"v_mfma_f32_16x16x32_bf16 v[84:87], a[172:175], v[44:47], v[84:87]\n"
"v_lshlrev_b32 v18, 16, v245\n"
"v_mfma_f32_16x16x32_bf16 v[72:75], a[176:179], v[44:47], v[72:75]\n"
"ds_read_b64_tr_b16 a[204:205], v5 offset:8200\n"
"v_fma_f32 v18, -v249, v10, v18\n"
"v_mfma_f32_16x16x32_bf16 v[76:79], a[180:183], v[44:47], v[76:79]\n"
"s_add_u32 m0, s62, 81920\n"
"v_mul_f32 v10, s83, v10\n"
"v_mfma_f32_16x16x32_bf16 v[64:67], a[184:187], v[44:47], v[64:67]\n"
"buffer_load_dwordx4 v4, s[48:51], s77 offen lds\n"
"v_mfma_f32_16x16x32_bf16 v[68:71], a[188:191], v[44:47], v[68:71]\n"
"ds_read_b64_tr_b16 a[206:207], v5 offset:9224\n"
"s_waitcnt lgkmcnt(8)\n"
"v_mfma_f32_16x16x32_bf16 v[88:91], a[168:171], v[32:35], v[88:91]\n"
"ds_read_b128 v[44:47], v7 offset:7232\n"
"v_mfma_f32_16x16x32_bf16 v[92:95], a[172:175], v[32:35], v[92:95]\n"
"v_mul_f32 v18, s83, v18\n"
"v_mfma_f32_16x16x32_bf16 v[80:83], a[176:179], v[32:35], v[80:83]\n"
"ds_read_b64_tr_b16 a[208:209], v5 offset:16384\n"
"v_and_b32 v11, 0xffff0000, v237\n"
"v_mfma_f32_16x16x32_bf16 v[84:87], a[180:183], v[32:35], v[84:87]\n"
"s_add_u32 m0, s62, 86016\n"
"v_mfma_f32_16x16x32_bf16 v[72:75], a[184:187], v[32:35], v[72:75]\n"
"buffer_load_dwordx4 v4, s[48:51], s78 offen lds\n"
"v_mfma_f32_16x16x32_bf16 v[76:79], a[188:191], v[32:35], v[76:79]\n"
"ds_read_b64_tr_b16 a[210:211], v5 offset:17408\n"
"s_waitcnt lgkmcnt(8)\n"
"v_mfma_f32_16x16x32_bf16 v[96:99], a[168:171], v[36:39], v[96:99]\n"
"ds_read_b128 v[32:35], v7 offset:16448\n"
"v_add_f32 v11, 0x3727c5ac, v11\n"
"v_mfma_f32_16x16x32_bf16 v[100:103], a[172:175], v[36:39], v[100:103]\n"
"v_rsq_f32 v11, v11\n"
"v_mfma_f32_16x16x32_bf16 v[88:91], a[176:179], v[36:39], v[88:91]\n"
"ds_read_b64_tr_b16 a[212:213], v5 offset:16392\n"
"v_mfma_f32_16x16x32_bf16 v[92:95], a[180:183], v[36:39], v[92:95]\n"
"s_add_u32 m0, s62, 90112\n"
"v_mfma_f32_16x16x32_bf16 v[80:83], a[184:187], v[36:39], v[80:83]\n"
"v_and_b32 v249, 0xffff0000, v241\n"
"v_mfma_f32_16x16x32_bf16 v[84:87], a[188:191], v[36:39], v[84:87]\n"
"ds_read_b64_tr_b16 a[214:215], v5 offset:17416\n"
"s_waitcnt lgkmcnt(8)\n"
"v_mfma_f32_16x16x32_bf16 v[104:107], a[168:171], v[40:43], v[104:107]\n"
"ds_read_b128 v[36:39], v7 offset:17472\n"
"v_mul_f32 v11, v11, v249\n"
"v_mfma_f32_16x16x32_bf16 v[108:111], a[172:175], v[40:43], v[108:111]\n"
"buffer_load_dwordx4 v4, s[52:55], s77 offen lds\n"
"v_mfma_f32_16x16x32_bf16 v[96:99], a[176:179], v[40:43], v[96:99]\n"
"v_and_b32 v249, 0xffff0000, v233\n"
"v_mfma_f32_16x16x32_bf16 v[100:103], a[180:183], v[40:43], v[100:103]\n"
"s_add_u32 m0, s62, 94208\n"
"v_and_b32 v19, 0xffff0000, v245\n"
"v_mfma_f32_16x16x32_bf16 v[88:91], a[184:187], v[40:43], v[88:91]\n"
"buffer_load_dwordx4 v4, s[52:55], s78 offen lds\n"
"v_mfma_f32_16x16x32_bf16 v[92:95], a[188:191], v[40:43], v[92:95]\n"
"s_waitcnt lgkmcnt(6)\n"
"v_mfma_f32_16x16x32_bf16 v[112:115], a[168:171], v[44:47], v[112:115]\n"
"ds_read_b128 v[40:43], v7 offset:18496\n"
"v_fma_f32 v19, -v249, v11, v19\n"
"v_mfma_f32_16x16x32_bf16 v[116:119], a[172:175], v[44:47], v[116:119]\n"
"v_mul_f32 v11, s83, v11\n"
"v_mfma_f32_16x16x32_bf16 v[104:107], a[176:179], v[44:47], v[104:107]\n"
"v_mul_f32 v19, s83, v19\n"
"v_mfma_f32_16x16x32_bf16 v[108:111], a[180:183], v[44:47], v[108:111]\n"
"v_mfma_f32_16x16x32_bf16 v[96:99], a[184:187], v[44:47], v[96:99]\n"
"v_mfma_f32_16x16x32_bf16 v[100:103], a[188:191], v[44:47], v[100:103]\n"
"s_waitcnt vmcnt(16) lgkmcnt(2)\n"
"s_barrier\n"
"v_mfma_f32_16x16x32_bf16 v[56:59], a[192:195], v[32:35], v[56:59]\n"
"ds_read_b128 v[44:47], v7 offset:19520\n"
"ds_read_b64_tr_b16 a[216:217], v5 offset:24576\n"
"v_mfma_f32_16x16x32_bf16 v[60:63], a[196:199], v[32:35], v[60:63]\n"
"s_lshl_b32 s77, s74, 9\n"
"s_add_u32 s78, s77, 16384\n"
"v_mfma_f32_16x16x32_bf16 v[48:51], a[200:203], v[32:35], v[48:51]\n"
"s_add_u32 m0, s62, 0\n"
"v_mfma_f32_16x16x32_bf16 v[52:55], a[204:207], v[32:35], v[52:55]\n"
"ds_read_b64_tr_b16 a[218:219], v5 offset:25600\n"
"s_waitcnt lgkmcnt(4)\n"
"v_mfma_f32_16x16x32_bf16 v[64:67], a[192:195], v[36:39], v[64:67]\n"
"ds_read_b128 v[32:35], v7 offset:20544\n"
"v_mfma_f32_16x16x32_bf16 v[68:71], a[196:199], v[36:39], v[68:71]\n"
"buffer_load_dwordx4 v4, s[44:47], s77 offen lds\n"
"v_mfma_f32_16x16x32_bf16 v[56:59], a[200:203], v[36:39], v[56:59]\n"
"ds_read_b64_tr_b16 a[220:221], v5 offset:24584\n"
"v_mfma_f32_16x16x32_bf16 v[60:63], a[204:207], v[36:39], v[60:63]\n"
"s_add_u32 m0, s62, 4096\n"
"v_mfma_f32_16x16x32_bf16 v[48:51], a[208:211], v[36:39], v[48:51]\n"
"buffer_load_dwordx4 v4, s[44:47], s78 offen lds\n"
"v_mfma_f32_16x16x32_bf16 v[52:55], a[212:215], v[36:39], v[52:55]\n"
"ds_read_b64_tr_b16 a[222:223], v5 offset:25608\n"
"s_waitcnt lgkmcnt(6)\n"
"v_mfma_f32_16x16x32_bf16 v[72:75], a[192:195], v[40:43], v[72:75]\n"
"ds_read_b128 v[36:39], v7 offset:21568\n"
"v_mfma_f32_16x16x32_bf16 v[76:79], a[196:199], v[40:43], v[76:79]\n"
"s_add_u32 m0, s62, 8192\n"
"v_mfma_f32_16x16x32_bf16 v[64:67], a[200:203], v[40:43], v[64:67]\n"
"ds_read_b64_tr_b16 a[224:225], v5 offset:32768\n"
"v_mfma_f32_16x16x32_bf16 v[68:71], a[204:207], v[40:43], v[68:71]\n"
"buffer_load_dwordx4 v4, s[48:51], s77 offen lds\n"
"v_mfma_f32_16x16x32_bf16 v[56:59], a[208:211], v[40:43], v[56:59]\n"
"s_add_u32 m0, s62, 12288\n"
"v_mfma_f32_16x16x32_bf16 v[60:63], a[212:215], v[40:43], v[60:63]\n"
"ds_read_b64_tr_b16 a[226:227], v5 offset:33792\n"
"s_waitcnt lgkmcnt(8)\n"
"v_mfma_f32_16x16x32_bf16 v[80:83], a[192:195], v[44:47], v[80:83]\n"
"ds_read_b128 v[40:43], v7 offset:22592\n"
"v_mfma_f32_16x16x32_bf16 v[84:87], a[196:199], v[44:47], v[84:87]\n"
"buffer_load_dwordx4 v4, s[48:51], s78 offen lds\n"
"v_mfma_f32_16x16x32_bf16 v[72:75], a[200:203], v[44:47], v[72:75]\n"
"ds_read_b64_tr_b16 a[228:229], v5 offset:32776\n"
"s_add_u32 m0, s62, 16384\n"
"v_mfma_f32_16x16x32_bf16 v[76:79], a[204:207], v[44:47], v[76:79]\n"
"buffer_load_dwordx4 v4, s[52:55], s77 offen lds\n"
"v_mfma_f32_16x16x32_bf16 v[64:67], a[208:211], v[44:47], v[64:67]\n"
"v_mfma_f32_16x16x32_bf16 v[68:71], a[212:215], v[44:47], v[68:71]\n"
"ds_read_b64_tr_b16 a[230:231], v5 offset:33800\n"
"s_waitcnt lgkmcnt(8)\n"
"v_mfma_f32_16x16x32_bf16 v[88:91], a[192:195], v[32:35], v[88:91]\n"
"ds_read_b128 v[44:47], v7 offset:23616\n"
"s_add_u32 m0, s62, 20480\n"
"v_mfma_f32_16x16x32_bf16 v[92:95], a[196:199], v[32:35], v[92:95]\n"
"buffer_load_dwordx4 v4, s[52:55], s78 offen lds\n"
"v_mfma_f32_16x16x32_bf16 v[80:83], a[200:203], v[32:35], v[80:83]\n"
"ds_read_b64_tr_b16 a[232:233], v5 offset:40960\n"
"v_mfma_f32_16x16x32_bf16 v[84:87], a[204:207], v[32:35], v[84:87]\n"
"v_mfma_f32_16x16x32_bf16 v[72:75], a[208:211], v[32:35], v[72:75]\n"
"s_add_u32 s77, s74, s76\n"
"s_add_u32 s78, s77, 4096\n"
"v_mfma_f32_16x16x32_bf16 v[76:79], a[212:215], v[32:35], v[76:79]\n"
"ds_read_b64_tr_b16 a[234:235], v5 offset:41984\n"
"s_waitcnt lgkmcnt(8)\n"
"v_mfma_f32_16x16x32_bf16 v[96:99], a[192:195], v[36:39], v[96:99]\n"
"ds_read_b128 v[32:35], v7 offset:16512\n"
"v_mfma_f32_16x16x32_bf16 v[100:103], a[196:199], v[36:39], v[100:103]\n"
"s_add_u32 m0, s63, 0\n"
"v_mfma_f32_16x16x32_bf16 v[88:91], a[200:203], v[36:39], v[88:91]\n"
"ds_read_b64_tr_b16 a[236:237], v5 offset:40968\n"
"v_mfma_f32_16x16x32_bf16 v[92:95], a[204:207], v[36:39], v[92:95]\n"
"buffer_load_dwordx4 v2, s[36:39], s77 offen nt lds\n"
"v_mfma_f32_16x16x32_bf16 v[80:83], a[208:211], v[36:39], v[80:83]\n"
"s_add_u32 m0, s63, 8256\n"
"v_mfma_f32_16x16x32_bf16 v[84:87], a[212:215], v[36:39], v[84:87]\n"
"ds_read_b64_tr_b16 a[238:239], v5 offset:41992\n"
"s_waitcnt lgkmcnt(8)\n"
"v_mfma_f32_16x16x32_bf16 v[104:107], a[192:195], v[40:43], v[104:107]\n"
"ds_read_b128 v[36:39], v7 offset:17536\n"
"v_mfma_f32_16x16x32_bf16 v[108:111], a[196:199], v[40:43], v[108:111]\n"
"buffer_load_dwordx4 v2, s[40:43], s77 offen nt lds\n"
"v_mfma_f32_16x16x32_bf16 v[96:99], a[200:203], v[40:43], v[96:99]\n"
"s_add_u32 m0, s63, 4096\n"
"v_mfma_f32_16x16x32_bf16 v[100:103], a[204:207], v[40:43], v[100:103]\n"
"buffer_load_dwordx4 v2, s[36:39], s78 offen nt lds\n"
"v_mfma_f32_16x16x32_bf16 v[88:91], a[208:211], v[40:43], v[88:91]\n"
"s_add_u32 m0, s63, 12352\n"
"v_mfma_f32_16x16x32_bf16 v[92:95], a[212:215], v[40:43], v[92:95]\n"
"s_waitcnt lgkmcnt(6)\n"
"v_mfma_f32_16x16x32_bf16 v[112:115], a[192:195], v[44:47], v[112:115]\n"
"ds_read_b128 v[40:43], v7 offset:18560\n"
"v_mfma_f32_16x16x32_bf16 v[116:119], a[196:199], v[44:47], v[116:119]\n"
"buffer_load_dwordx4 v2, s[40:43], s78 offen nt lds\n"
"v_mfma_f32_16x16x32_bf16 v[104:107], a[200:203], v[44:47], v[104:107]\n"
"v_mfma_f32_16x16x32_bf16 v[108:111], a[204:207], v[44:47], v[108:111]\n"
"v_mfma_f32_16x16x32_bf16 v[96:99], a[208:211], v[44:47], v[96:99]\n"
"v_mfma_f32_16x16x32_bf16 v[100:103], a[212:215], v[44:47], v[100:103]\n"
"s_waitcnt vmcnt(16) lgkmcnt(2)\n"
"s_barrier\n"
"v_mfma_f32_16x16x32_bf16 v[56:59], a[216:219], v[32:35], v[56:59]\n"
"ds_read_b128 v[44:47], v7 offset:19584\n"
"ds_read_b64_tr_b16 a[240:241], v6 offset:0\n"
"v_mfma_f32_16x16x32_bf16 v[60:63], a[220:223], v[32:35], v[60:63]\n"
"s_lshl_b32 s77, s74, 9\n"
"s_mov_b32 s68, s81\n"
"v_mfma_f32_16x16x32_bf16 v[48:51], a[224:227], v[32:35], v[48:51]\n"
"s_add_u32 s77, s77, 32768\n"
"v_lshlrev_b32 v12, 16, v238\n"
"v_mfma_f32_16x16x32_bf16 v[52:55], a[228:231], v[32:35], v[52:55]\n"
"ds_read_b64_tr_b16 a[242:243], v6 offset:1024\n"
"s_waitcnt lgkmcnt(4)\n"
"v_mfma_f32_16x16x32_bf16 v[64:67], a[216:219], v[36:39], v[64:67]\n"
"ds_read_b128 v[32:35], v7 offset:20608\n"
"v_add_f32 v12, 0x3727c5ac, v12\n"
"v_mfma_f32_16x16x32_bf16 v[68:71], a[220:223], v[36:39], v[68:71]\n"
"s_add_u32 s78, s77, 16384\n"
"v_mfma_f32_16x16x32_bf16 v[56:59], a[224:227], v[36:39], v[56:59]\n"
"ds_read_b64_tr_b16 a[244:245], v6 offset:8\n"
"v_mfma_f32_16x16x32_bf16 v[60:63], a[228:231], v[36:39], v[60:63]\n"
"s_add_u32 m0, s62, 24576\n"
"v_mfma_f32_16x16x32_bf16 v[48:51], a[232:235], v[36:39], v[48:51]\n"
"buffer_load_dwordx4 v4, s[44:47], s77 offen lds\n"
"v_mfma_f32_16x16x32_bf16 v[52:55], a[236:239], v[36:39], v[52:55]\n"
"ds_read_b64_tr_b16 a[246:247], v6 offset:1032\n"
"s_waitcnt lgkmcnt(6)\n"
"v_mfma_f32_16x16x32_bf16 v[72:75], a[216:219], v[40:43], v[72:75]\n"
"ds_read_b128 v[36:39], v7 offset:21632\n"
"v_mfma_f32_16x16x32_bf16 v[76:79], a[220:223], v[40:43], v[76:79]\n"
"v_rsq_f32 v12, v12\n"
"v_mfma_f32_16x16x32_bf16 v[64:67], a[224:227], v[40:43], v[64:67]\n"
"ds_read_b64_tr_b16 a[248:249], v6 offset:8192\n"
"s_add_u32 s69, s81, 148032\n"
"v_mfma_f32_16x16x32_bf16 v[68:71], a[228:231], v[40:43], v[68:71]\n"
"s_add_u32 m0, s62, 28672\n"
"v_lshlrev_b32 v249, 16, v242\n"
"v_mfma_f32_16x16x32_bf16 v[56:59], a[232:235], v[40:43], v[56:59]\n"
"buffer_load_dwordx4 v4, s[44:47], s78 offen lds\n"
"v_mfma_f32_16x16x32_bf16 v[60:63], a[236:239], v[40:43], v[60:63]\n"
"ds_read_b64_tr_b16 a[250:251], v6 offset:9216\n"
"s_waitcnt lgkmcnt(8)\n"
"v_mfma_f32_16x16x32_bf16 v[80:83], a[216:219], v[44:47], v[80:83]\n"
"ds_read_b128 v[40:43], v7 offset:22656\n"
"v_mul_f32 v12, v12, v249\n"
"v_mfma_f32_16x16x32_bf16 v[84:87], a[220:223], v[44:47], v[84:87]\n"
"v_lshlrev_b32 v249, 16, v234\n"
"v_lshlrev_b32 v20, 16, v246\n"
"v_mfma_f32_16x16x32_bf16 v[72:75], a[224:227], v[44:47], v[72:75]\n"
"ds_read_b64_tr_b16 a[252:253], v6 offset:8200\n"
"v_fma_f32 v20, -v249, v12, v20\n"
"v_mfma_f32_16x16x32_bf16 v[76:79], a[228:231], v[44:47], v[76:79]\n"
"s_add_u32 m0, s62, 32768\n"
"v_mfma_f32_16x16x32_bf16 v[64:67], a[232:235], v[44:47], v[64:67]\n"
"buffer_load_dwordx4 v4, s[48:51], s77 offen lds\n"
"v_mfma_f32_16x16x32_bf16 v[68:71], a[236:239], v[44:47], v[68:71]\n"
"ds_read_b64_tr_b16 a[254:255], v6 offset:9224\n"
"s_waitcnt lgkmcnt(8)\n"
"v_mfma_f32_16x16x32_bf16 v[88:91], a[216:219], v[32:35], v[88:91]\n"
"ds_read_b128 v[44:47], v7 offset:23680\n"
"v_mul_f32 v12, s83, v12\n"
"v_mfma_f32_16x16x32_bf16 v[92:95], a[220:223], v[32:35], v[92:95]\n"
"v_mul_f32 v20, s83, v20\n"
"v_mfma_f32_16x16x32_bf16 v[80:83], a[224:227], v[32:35], v[80:83]\n"
"ds_read_b64_tr_b16 v[128:129], v6 offset:16384\n"
"s_mov_b32 s70, 0\n"
"v_mfma_f32_16x16x32_bf16 v[84:87], a[228:231], v[32:35], v[84:87]\n"
"s_add_u32 m0, s62, 36864\n"
"v_and_b32 v13, 0xffff0000, v238\n"
"v_mfma_f32_16x16x32_bf16 v[72:75], a[232:235], v[32:35], v[72:75]\n"
"buffer_load_dwordx4 v4, s[48:51], s78 offen lds\n"
"v_mfma_f32_16x16x32_bf16 v[76:79], a[236:239], v[32:35], v[76:79]\n"
"ds_read_b64_tr_b16 v[130:131], v6 offset:17408\n"
"s_waitcnt lgkmcnt(8)\n"
"v_mfma_f32_16x16x32_bf16 v[96:99], a[216:219], v[36:39], v[96:99]\n"
"ds_read_b128 v[32:35], v7 offset:32896\n"
"v_mfma_f32_16x16x32_bf16 v[100:103], a[220:223], v[36:39], v[100:103]\n"
"v_add_f32 v13, 0x3727c5ac, v13\n"
"v_mfma_f32_16x16x32_bf16 v[88:91], a[224:227], v[36:39], v[88:91]\n"
"ds_read_b64_tr_b16 v[132:133], v6 offset:16392\n"
"v_mfma_f32_16x16x32_bf16 v[92:95], a[228:231], v[36:39], v[92:95]\n"
"s_add_u32 m0, s62, 40960\n"
"v_mfma_f32_16x16x32_bf16 v[80:83], a[232:235], v[36:39], v[80:83]\n"
"v_rsq_f32 v13, v13\n"
"v_mfma_f32_16x16x32_bf16 v[84:87], a[236:239], v[36:39], v[84:87]\n"
"ds_read_b64_tr_b16 v[134:135], v6 offset:17416\n"
"s_waitcnt lgkmcnt(8)\n"
"v_mfma_f32_16x16x32_bf16 v[104:107], a[216:219], v[40:43], v[104:107]\n"
"ds_read_b128 v[36:39], v7 offset:33920\n"
"s_mov_b32 s71, 4\n"
"v_mfma_f32_16x16x32_bf16 v[108:111], a[220:223], v[40:43], v[108:111]\n"
"buffer_load_dwordx4 v4, s[52:55], s77 offen lds\n"
"v_mfma_f32_16x16x32_bf16 v[96:99], a[224:227], v[40:43], v[96:99]\n"
"v_and_b32 v249, 0xffff0000, v242\n"
"v_mul_f32 v13, v13, v249\n"
"v_mfma_f32_16x16x32_bf16 v[100:103], a[228:231], v[40:43], v[100:103]\n"
"s_add_u32 m0, s62, 45056\n"
"v_and_b32 v249, 0xffff0000, v234\n"
"v_mfma_f32_16x16x32_bf16 v[88:91], a[232:235], v[40:43], v[88:91]\n"
"buffer_load_dwordx4 v4, s[52:55], s78 offen lds\n"
"v_mfma_f32_16x16x32_bf16 v[92:95], a[236:239], v[40:43], v[92:95]\n"
"v_and_b32 v21, 0xffff0000, v246\n"
"s_waitcnt lgkmcnt(6)\n"
"v_mfma_f32_16x16x32_bf16 v[112:115], a[216:219], v[44:47], v[112:115]\n"
"ds_read_b128 v[40:43], v7 offset:34944\n"
"v_mfma_f32_16x16x32_bf16 v[116:119], a[220:223], v[44:47], v[116:119]\n"
"v_fma_f32 v21, -v249, v13, v21\n"
"v_mul_f32 v13, s83, v13\n"
"v_mfma_f32_16x16x32_bf16 v[104:107], a[224:227], v[44:47], v[104:107]\n"
"v_mul_f32 v21, s83, v21\n"
"v_mfma_f32_16x16x32_bf16 v[108:111], a[228:231], v[44:47], v[108:111]\n"
"v_mfma_f32_16x16x32_bf16 v[96:99], a[232:235], v[44:47], v[96:99]\n"
"v_mfma_f32_16x16x32_bf16 v[100:103], a[236:239], v[44:47], v[100:103]\n"
"s_waitcnt vmcnt(16) lgkmcnt(2)\n"
"s_barrier\n"
"v_mfma_f32_16x16x32_bf16 v[56:59], a[240:243], v[32:35], v[56:59]\n"
"ds_read_b128 v[44:47], v7 offset:35968\n"
"ds_read_b64_tr_b16 v[136:137], v6 offset:24576\n"
"v_mfma_f32_16x16x32_bf16 v[60:63], a[244:247], v[32:35], v[60:63]\n"
"s_lshl_b32 s77, s75, 9\n"
"s_add_u32 s78, s77, 16384\n"
"v_mfma_f32_16x16x32_bf16 v[48:51], a[248:251], v[32:35], v[48:51]\n"
"s_add_u32 m0, s62, 49152\n"
"v_mfma_f32_16x16x32_bf16 v[52:55], a[252:255], v[32:35], v[52:55]\n"
"ds_read_b64_tr_b16 v[138:139], v6 offset:25600\n"
"s_waitcnt lgkmcnt(4)\n"
"v_mfma_f32_16x16x32_bf16 v[64:67], a[240:243], v[36:39], v[64:67]\n"
"ds_read_b128 v[32:35], v7 offset:36992\n"
"v_mfma_f32_16x16x32_bf16 v[68:71], a[244:247], v[36:39], v[68:71]\n"
"buffer_load_dwordx4 v4, s[44:47], s77 offen lds\n"
"v_mfma_f32_16x16x32_bf16 v[56:59], a[248:251], v[36:39], v[56:59]\n"
"ds_read_b64_tr_b16 v[140:141], v6 offset:24584\n"
"v_mfma_f32_16x16x32_bf16 v[60:63], a[252:255], v[36:39], v[60:63]\n"
"s_add_u32 m0, s62, 53248\n"
"v_mfma_f32_16x16x32_bf16 v[48:51], v[128:131], v[36:39], v[48:51]\n"
"buffer_load_dwordx4 v4, s[44:47], s78 offen lds\n"
"v_mfma_f32_16x16x32_bf16 v[52:55], v[132:135], v[36:39], v[52:55]\n"
"ds_read_b64_tr_b16 v[142:143], v6 offset:25608\n"
"s_waitcnt lgkmcnt(6)\n"
"v_mfma_f32_16x16x32_bf16 v[72:75], a[240:243], v[40:43], v[72:75]\n"
"ds_read_b128 v[36:39], v7 offset:38016\n"
"v_mfma_f32_16x16x32_bf16 v[76:79], a[244:247], v[40:43], v[76:79]\n"
"s_add_u32 m0, s62, 57344\n"
"v_mfma_f32_16x16x32_bf16 v[64:67], a[248:251], v[40:43], v[64:67]\n"
"ds_read_b64_tr_b16 v[144:145], v6 offset:32768\n"
"v_mfma_f32_16x16x32_bf16 v[68:71], a[252:255], v[40:43], v[68:71]\n"
"buffer_load_dwordx4 v4, s[48:51], s77 offen lds\n"
"v_mfma_f32_16x16x32_bf16 v[56:59], v[128:131], v[40:43], v[56:59]\n"
"s_add_u32 m0, s62, 61440\n"
"v_mfma_f32_16x16x32_bf16 v[60:63], v[132:135], v[40:43], v[60:63]\n"
"ds_read_b64_tr_b16 v[146:147], v6 offset:33792\n"
"s_waitcnt lgkmcnt(8)\n"
"v_mfma_f32_16x16x32_bf16 v[80:83], a[240:243], v[44:47], v[80:83]\n"
"ds_read_b128 v[40:43], v7 offset:39040\n"
"v_mfma_f32_16x16x32_bf16 v[84:87], a[244:247], v[44:47], v[84:87]\n"
"buffer_load_dwordx4 v4, s[48:51], s78 offen lds\n"
"v_mfma_f32_16x16x32_bf16 v[72:75], a[248:251], v[44:47], v[72:75]\n"
"ds_read_b64_tr_b16 v[148:149], v6 offset:32776\n"
"s_add_u32 m0, s62, 65536\n"
"v_mfma_f32_16x16x32_bf16 v[76:79], a[252:255], v[44:47], v[76:79]\n"
"buffer_load_dwordx4 v4, s[52:55], s77 offen lds\n"
"v_mfma_f32_16x16x32_bf16 v[64:67], v[128:131], v[44:47], v[64:67]\n"
"v_mfma_f32_16x16x32_bf16 v[68:71], v[132:135], v[44:47], v[68:71]\n"
"ds_read_b64_tr_b16 v[150:151], v6 offset:33800\n"
"s_waitcnt lgkmcnt(8)\n"
"v_mfma_f32_16x16x32_bf16 v[88:91], a[240:243], v[32:35], v[88:91]\n"
"ds_read_b128 v[44:47], v7 offset:40064\n"
"s_add_u32 m0, s62, 69632\n"
"v_mfma_f32_16x16x32_bf16 v[92:95], a[244:247], v[32:35], v[92:95]\n"
"buffer_load_dwordx4 v4, s[52:55], s78 offen lds\n"
"v_mfma_f32_16x16x32_bf16 v[80:83], a[248:251], v[32:35], v[80:83]\n"
"ds_read_b64_tr_b16 v[152:153], v6 offset:40960\n"
"v_mfma_f32_16x16x32_bf16 v[84:87], a[252:255], v[32:35], v[84:87]\n"
"v_mfma_f32_16x16x32_bf16 v[72:75], v[128:131], v[32:35], v[72:75]\n"
"s_add_u32 s77, s75, s76\n"
"s_add_u32 s78, s77, 4096\n"
"v_mfma_f32_16x16x32_bf16 v[76:79], v[132:135], v[32:35], v[76:79]\n"
"ds_read_b64_tr_b16 v[154:155], v6 offset:41984\n"
"s_waitcnt lgkmcnt(8)\n"
"v_mfma_f32_16x16x32_bf16 v[96:99], a[240:243], v[36:39], v[96:99]\n"
"ds_read_b128 v[32:35], v7 offset:32960\n"
"v_mfma_f32_16x16x32_bf16 v[100:103], a[244:247], v[36:39], v[100:103]\n"
"s_add_u32 m0, s63, 16448\n"
"v_mfma_f32_16x16x32_bf16 v[88:91], a[248:251], v[36:39], v[88:91]\n"
"ds_read_b64_tr_b16 v[156:157], v6 offset:40968\n"
"v_mfma_f32_16x16x32_bf16 v[92:95], a[252:255], v[36:39], v[92:95]\n"
"buffer_load_dwordx4 v2, s[36:39], s77 offen nt lds\n"
"v_mfma_f32_16x16x32_bf16 v[80:83], v[128:131], v[36:39], v[80:83]\n"
"s_add_u32 m0, s63, 24704\n"
"v_mfma_f32_16x16x32_bf16 v[84:87], v[132:135], v[36:39], v[84:87]\n"
"ds_read_b64_tr_b16 v[158:159], v6 offset:41992\n"
"s_waitcnt lgkmcnt(8)\n"
"v_mfma_f32_16x16x32_bf16 v[104:107], a[240:243], v[40:43], v[104:107]\n"
"ds_read_b128 v[36:39], v7 offset:33984\n"
"v_mfma_f32_16x16x32_bf16 v[108:111], a[244:247], v[40:43], v[108:111]\n"
"buffer_load_dwordx4 v2, s[40:43], s77 offen nt lds\n"
"v_mfma_f32_16x16x32_bf16 v[96:99], a[248:251], v[40:43], v[96:99]\n"
"s_add_u32 m0, s63, 20544\n"
"v_mfma_f32_16x16x32_bf16 v[100:103], a[252:255], v[40:43], v[100:103]\n"
"buffer_load_dwordx4 v2, s[36:39], s78 offen nt lds\n"
"v_mfma_f32_16x16x32_bf16 v[88:91], v[128:131], v[40:43], v[88:91]\n"
"s_add_u32 m0, s63, 28800\n"
"v_mfma_f32_16x16x32_bf16 v[92:95], v[132:135], v[40:43], v[92:95]\n"
"s_waitcnt lgkmcnt(6)\n"
"v_mfma_f32_16x16x32_bf16 v[112:115], a[240:243], v[44:47], v[112:115]\n"
"ds_read_b128 v[40:43], v7 offset:35008\n"
"v_mfma_f32_16x16x32_bf16 v[116:119], a[244:247], v[44:47], v[116:119]\n"
"buffer_load_dwordx4 v2, s[40:43], s78 offen nt lds\n"
"v_mfma_f32_16x16x32_bf16 v[104:107], a[248:251], v[44:47], v[104:107]\n"
"v_mfma_f32_16x16x32_bf16 v[108:111], a[252:255], v[44:47], v[108:111]\n"
"v_mfma_f32_16x16x32_bf16 v[96:99], v[128:131], v[44:47], v[96:99]\n"
"v_mfma_f32_16x16x32_bf16 v[100:103], v[132:135], v[44:47], v[100:103]\n"
"s_waitcnt vmcnt(16) lgkmcnt(2)\n"
"s_barrier\n"
"v_mfma_f32_16x16x32_bf16 v[56:59], v[136:139], v[32:35], v[56:59]\n"
"ds_read_b128 v[44:47], v7 offset:36032\n"
"ds_read_b64_tr_b16 v[160:161], v5 offset:0\n"
"v_mfma_f32_16x16x32_bf16 v[60:63], v[140:143], v[32:35], v[60:63]\n"
"s_lshl_b32 s77, s75, 9\n"
"v_lshlrev_b32 v14, 16, v239\n"
"v_mfma_f32_16x16x32_bf16 v[48:51], v[144:147], v[32:35], v[48:51]\n"
"s_add_u32 s77, s77, 32768\n"
"v_add_f32 v14, 0x3727c5ac, v14\n"
"v_mfma_f32_16x16x32_bf16 v[52:55], v[148:151], v[32:35], v[52:55]\n"
"ds_read_b64_tr_b16 v[162:163], v5 offset:1024\n"
"s_waitcnt lgkmcnt(4)\n"
"v_mfma_f32_16x16x32_bf16 v[64:67], v[136:139], v[36:39], v[64:67]\n"
"ds_read_b128 v[32:35], v7 offset:37056\n"
"v_mfma_f32_16x16x32_bf16 v[68:71], v[140:143], v[36:39], v[68:71]\n"
"s_add_u32 s78, s77, 16384\n"
"v_mfma_f32_16x16x32_bf16 v[56:59], v[144:147], v[36:39], v[56:59]\n"
"ds_read_b64_tr_b16 v[164:165], v5 offset:8\n"
"v_mfma_f32_16x16x32_bf16 v[60:63], v[148:151], v[36:39], v[60:63]\n"
"s_add_u32 m0, s62, 73728\n"
"v_mfma_f32_16x16x32_bf16 v[48:51], v[152:155], v[36:39], v[48:51]\n"
"buffer_load_dwordx4 v4, s[44:47], s77 offen lds\n"
"v_mfma_f32_16x16x32_bf16 v[52:55], v[156:159], v[36:39], v[52:55]\n"
"ds_read_b64_tr_b16 v[166:167], v5 offset:1032\n"
"s_waitcnt lgkmcnt(6)\n"
"v_mfma_f32_16x16x32_bf16 v[72:75], v[136:139], v[40:43], v[72:75]\n"
"ds_read_b128 v[36:39], v7 offset:38080\n"
"v_mfma_f32_16x16x32_bf16 v[76:79], v[140:143], v[40:43], v[76:79]\n"
"v_rsq_f32 v14, v14\n"
"v_mfma_f32_16x16x32_bf16 v[64:67], v[144:147], v[40:43], v[64:67]\n"
"ds_read_b64_tr_b16 v[168:169], v5 offset:8192\n"
"v_lshlrev_b32 v249, 16, v243\n"
"v_mfma_f32_16x16x32_bf16 v[68:71], v[148:151], v[40:43], v[68:71]\n"
"s_add_u32 m0, s62, 77824\n"
"v_mul_f32 v14, v14, v249\n"
"v_mfma_f32_16x16x32_bf16 v[56:59], v[152:155], v[40:43], v[56:59]\n"
"buffer_load_dwordx4 v4, s[44:47], s78 offen lds\n"
"v_mfma_f32_16x16x32_bf16 v[60:63], v[156:159], v[40:43], v[60:63]\n"
"ds_read_b64_tr_b16 v[170:171], v5 offset:9216\n"
"s_waitcnt lgkmcnt(8)\n"
"v_mfma_f32_16x16x32_bf16 v[80:83], v[136:139], v[44:47], v[80:83]\n"
"ds_read_b128 v[40:43], v7 offset:39104\n"
"v_lshlrev_b32 v249, 16, v235\n"
"v_mfma_f32_16x16x32_bf16 v[84:87], v[140:143], v[44:47], v[84:87]\n"
"v_lshlrev_b32 v22, 16, v247\n"
"v_mfma_f32_16x16x32_bf16 v[72:75], v[144:147], v[44:47], v[72:75]\n"
"ds_read_b64_tr_b16 v[172:173], v5 offset:8200\n"
"v_fma_f32 v22, -v249, v14, v22\n"
"v_mfma_f32_16x16x32_bf16 v[76:79], v[148:151], v[44:47], v[76:79]\n"
"s_add_u32 m0, s62, 81920\n"
"v_mul_f32 v14, s83, v14\n"
"v_mfma_f32_16x16x32_bf16 v[64:67], v[152:155], v[44:47], v[64:67]\n"
"buffer_load_dwordx4 v4, s[48:51], s77 offen lds\n"
"v_mfma_f32_16x16x32_bf16 v[68:71], v[156:159], v[44:47], v[68:71]\n"
"ds_read_b64_tr_b16 v[174:175], v5 offset:9224\n"
"s_waitcnt lgkmcnt(8)\n"
"v_mfma_f32_16x16x32_bf16 v[88:91], v[136:139], v[32:35], v[88:91]\n"
"ds_read_b128 v[44:47], v7 offset:40128\n"
"v_mfma_f32_16x16x32_bf16 v[92:95], v[140:143], v[32:35], v[92:95]\n"
"v_mul_f32 v22, s83, v22\n"
"v_mfma_f32_16x16x32_bf16 v[80:83], v[144:147], v[32:35], v[80:83]\n"
"ds_read_b64_tr_b16 v[176:177], v5 offset:16384\n"
"v_and_b32 v15, 0xffff0000, v239\n"
"v_mfma_f32_16x16x32_bf16 v[84:87], v[148:151], v[32:35], v[84:87]\n"
"s_add_u32 m0, s62, 86016\n"
"v_mfma_f32_16x16x32_bf16 v[72:75], v[152:155], v[32:35], v[72:75]\n"
"buffer_load_dwordx4 v4, s[48:51], s78 offen lds\n"
"v_mfma_f32_16x16x32_bf16 v[76:79], v[156:159], v[32:35], v[76:79]\n"
"ds_read_b64_tr_b16 v[178:179], v5 offset:17408\n"
"s_waitcnt lgkmcnt(8)\n"
"v_mfma_f32_16x16x32_bf16 v[96:99], v[136:139], v[36:39], v[96:99]\n"
"ds_read_b128 v[32:35], v7 offset:0\n"
"v_add_f32 v15, 0x3727c5ac, v15\n"
"v_mfma_f32_16x16x32_bf16 v[100:103], v[140:143], v[36:39], v[100:103]\n"
"v_rsq_f32 v15, v15\n"
"v_mfma_f32_16x16x32_bf16 v[88:91], v[144:147], v[36:39], v[88:91]\n"
"ds_read_b64_tr_b16 v[180:181], v5 offset:16392\n"
"v_mfma_f32_16x16x32_bf16 v[92:95], v[148:151], v[36:39], v[92:95]\n"
"s_add_u32 m0, s62, 90112\n"
"v_mfma_f32_16x16x32_bf16 v[80:83], v[152:155], v[36:39], v[80:83]\n"
"v_and_b32 v249, 0xffff0000, v243\n"
"v_mfma_f32_16x16x32_bf16 v[84:87], v[156:159], v[36:39], v[84:87]\n"
"ds_read_b64_tr_b16 v[182:183], v5 offset:17416\n"
"s_waitcnt lgkmcnt(8)\n"
"v_mfma_f32_16x16x32_bf16 v[104:107], v[136:139], v[40:43], v[104:107]\n"
"ds_read_b128 v[36:39], v7 offset:1024\n"
"v_mul_f32 v15, v15, v249\n"
"v_mfma_f32_16x16x32_bf16 v[108:111], v[140:143], v[40:43], v[108:111]\n"
"buffer_load_dwordx4 v4, s[52:55], s77 offen lds\n"
"v_mfma_f32_16x16x32_bf16 v[96:99], v[144:147], v[40:43], v[96:99]\n"
"v_and_b32 v249, 0xffff0000, v235\n"
"v_mfma_f32_16x16x32_bf16 v[100:103], v[148:151], v[40:43], v[100:103]\n"
"s_add_u32 m0, s62, 94208\n"
"v_and_b32 v23, 0xffff0000, v247\n"
"v_mfma_f32_16x16x32_bf16 v[88:91], v[152:155], v[40:43], v[88:91]\n"
"buffer_load_dwordx4 v4, s[52:55], s78 offen lds\n"
"v_mfma_f32_16x16x32_bf16 v[92:95], v[156:159], v[40:43], v[92:95]\n"
"s_waitcnt lgkmcnt(6)\n"
"v_mfma_f32_16x16x32_bf16 v[112:115], v[136:139], v[44:47], v[112:115]\n"
"ds_read_b128 v[40:43], v7 offset:2048\n"
"v_fma_f32 v23, -v249, v15, v23\n"
"v_mfma_f32_16x16x32_bf16 v[116:119], v[140:143], v[44:47], v[116:119]\n"
"v_mul_f32 v15, s83, v15\n"
"v_mfma_f32_16x16x32_bf16 v[104:107], v[144:147], v[44:47], v[104:107]\n"
"v_mul_f32 v23, s83, v23\n"
"v_mfma_f32_16x16x32_bf16 v[108:111], v[148:151], v[44:47], v[108:111]\n"
"v_mfma_f32_16x16x32_bf16 v[96:99], v[152:155], v[44:47], v[96:99]\n"
"v_mfma_f32_16x16x32_bf16 v[100:103], v[156:159], v[44:47], v[100:103]\n"
"s_waitcnt vmcnt(16) lgkmcnt(2)\n"
"s_barrier\n"
"v_mfma_f32_16x16x32_bf16 v[56:59], v[160:163], v[32:35], v[56:59]\n"
"ds_read_b128 v[44:47], v7 offset:3072\n"
"ds_read_b64_tr_b16 v[184:185], v5 offset:24576\n"
"v_mfma_f32_16x16x32_bf16 v[60:63], v[164:167], v[32:35], v[60:63]\n"
"s_mov_b32 m0, s68\n"
"v_mfma_f32_16x16x32_bf16 v[48:51], v[168:171], v[32:35], v[48:51]\n"
"buffer_load_dwordx4 v2, s[36:39], s79 offen nt lds\n"
"v_mfma_f32_16x16x32_bf16 v[52:55], v[172:175], v[32:35], v[52:55]\n"
"ds_read_b64_tr_b16 v[186:187], v5 offset:25600\n"
"s_waitcnt lgkmcnt(4)\n"
"v_mfma_f32_16x16x32_bf16 v[64:67], v[160:163], v[36:39], v[64:67]\n"
"ds_read_b128 v[32:35], v7 offset:4096\n"
"v_mfma_f32_16x16x32_bf16 v[68:71], v[164:167], v[36:39], v[68:71]\n"
"v_mfma_f32_16x16x32_bf16 v[56:59], v[168:171], v[36:39], v[56:59]\n"
"ds_read_b64_tr_b16 v[188:189], v5 offset:24584\n"
"v_mfma_f32_16x16x32_bf16 v[60:63], v[172:175], v[36:39], v[60:63]\n"
"v_mfma_f32_16x16x32_bf16 v[48:51], v[176:179], v[36:39], v[48:51]\n"
"s_add_u32 m0, s68, 8256\n"
"v_mfma_f32_16x16x32_bf16 v[52:55], v[180:183], v[36:39], v[52:55]\n"
"ds_read_b64_tr_b16 v[190:191], v5 offset:25608\n"
"s_waitcnt lgkmcnt(6)\n"
"v_mfma_f32_16x16x32_bf16 v[72:75], v[160:163], v[40:43], v[72:75]\n"
"ds_read_b128 v[36:39], v7 offset:5120\n"
"v_mfma_f32_16x16x32_bf16 v[76:79], v[164:167], v[40:43], v[76:79]\n"
"buffer_load_dwordx4 v2, s[40:43], s79 offen nt lds\n"
"v_mfma_f32_16x16x32_bf16 v[64:67], v[168:171], v[40:43], v[64:67]\n"
"ds_read_b64_tr_b16 v[192:193], v5 offset:32768\n"
"v_mfma_f32_16x16x32_bf16 v[68:71], v[172:175], v[40:43], v[68:71]\n"
"v_mfma_f32_16x16x32_bf16 v[56:59], v[176:179], v[40:43], v[56:59]\n"
"v_mfma_f32_16x16x32_bf16 v[60:63], v[180:183], v[40:43], v[60:63]\n"
"ds_read_b64_tr_b16 v[194:195], v5 offset:33792\n"
"s_waitcnt lgkmcnt(8)\n"
"v_mfma_f32_16x16x32_bf16 v[80:83], v[160:163], v[44:47], v[80:83]\n"
"ds_read_b128 v[40:43], v7 offset:6144\n"
"s_add_u32 s79, s79, 1024\n"
"v_mfma_f32_16x16x32_bf16 v[84:87], v[164:167], v[44:47], v[84:87]\n"
"v_mfma_f32_16x16x32_bf16 v[72:75], v[168:171], v[44:47], v[72:75]\n"
"ds_read_b64_tr_b16 v[196:197], v5 offset:32776\n"
"s_add_u32 m0, s68, 1024\n"
"v_mfma_f32_16x16x32_bf16 v[76:79], v[172:175], v[44:47], v[76:79]\n"
"v_mfma_f32_16x16x32_bf16 v[64:67], v[176:179], v[44:47], v[64:67]\n"
"buffer_load_dwordx4 v2, s[36:39], s80 offen nt lds\n"
"v_mfma_f32_16x16x32_bf16 v[68:71], v[180:183], v[44:47], v[68:71]\n"
"ds_read_b64_tr_b16 v[198:199], v5 offset:33800\n"
"s_waitcnt lgkmcnt(8)\n"
"v_mfma_f32_16x16x32_bf16 v[88:91], v[160:163], v[32:35], v[88:91]\n"
"ds_read_b128 v[44:47], v7 offset:7168\n"
"v_mfma_f32_16x16x32_bf16 v[92:95], v[164:167], v[32:35], v[92:95]\n"
"v_mfma_f32_16x16x32_bf16 v[80:83], v[168:171], v[32:35], v[80:83]\n"
"ds_read_b64_tr_b16 v[200:201], v5 offset:40960\n"
"v_mfma_f32_16x16x32_bf16 v[84:87], v[172:175], v[32:35], v[84:87]\n"
"v_mfma_f32_16x16x32_bf16 v[72:75], v[176:179], v[32:35], v[72:75]\n"
"s_add_u32 m0, s68, 9280\n"
"v_mfma_f32_16x16x32_bf16 v[76:79], v[180:183], v[32:35], v[76:79]\n"
"ds_read_b64_tr_b16 v[202:203], v5 offset:41984\n"
"s_waitcnt lgkmcnt(8)\n"
"v_mfma_f32_16x16x32_bf16 v[96:99], v[160:163], v[36:39], v[96:99]\n"
"ds_read_b128 v[32:35], v7 offset:64\n"
"v_mfma_f32_16x16x32_bf16 v[100:103], v[164:167], v[36:39], v[100:103]\n"
"buffer_load_dwordx4 v2, s[40:43], s80 offen nt lds\n"
"v_mfma_f32_16x16x32_bf16 v[88:91], v[168:171], v[36:39], v[88:91]\n"
"ds_read_b64_tr_b16 v[204:205], v5 offset:40968\n"
"v_mfma_f32_16x16x32_bf16 v[92:95], v[172:175], v[36:39], v[92:95]\n"
"v_mfma_f32_16x16x32_bf16 v[80:83], v[176:179], v[36:39], v[80:83]\n"
"v_mfma_f32_16x16x32_bf16 v[84:87], v[180:183], v[36:39], v[84:87]\n"
"ds_read_b64_tr_b16 v[206:207], v5 offset:41992\n"
"s_waitcnt lgkmcnt(8)\n"
"v_mfma_f32_16x16x32_bf16 v[104:107], v[160:163], v[40:43], v[104:107]\n"
"ds_read_b128 v[36:39], v7 offset:1088\n"
"s_add_u32 s80, s80, 1024\n"
"v_mfma_f32_16x16x32_bf16 v[108:111], v[164:167], v[40:43], v[108:111]\n"
"v_mfma_f32_16x16x32_bf16 v[96:99], v[168:171], v[40:43], v[96:99]\n"
"s_add_u32 s68, s68, 16448\n"
"v_mfma_f32_16x16x32_bf16 v[100:103], v[172:175], v[40:43], v[100:103]\n"
"v_mfma_f32_16x16x32_bf16 v[88:91], v[176:179], v[40:43], v[88:91]\n"
"s_cmp_eq_u32 s68, s69\ns_cselect_b32 s68, s81, s68\n"
"v_mfma_f32_16x16x32_bf16 v[92:95], v[180:183], v[40:43], v[92:95]\n"
"s_waitcnt lgkmcnt(6)\n"
"v_mfma_f32_16x16x32_bf16 v[112:115], v[160:163], v[44:47], v[112:115]\n"
"ds_read_b128 v[40:43], v7 offset:2112\n"
"v_mfma_f32_16x16x32_bf16 v[116:119], v[164:167], v[44:47], v[116:119]\n"
"v_mfma_f32_16x16x32_bf16 v[104:107], v[168:171], v[44:47], v[104:107]\n"
"v_mfma_f32_16x16x32_bf16 v[108:111], v[172:175], v[44:47], v[108:111]\n"
"v_mfma_f32_16x16x32_bf16 v[96:99], v[176:179], v[44:47], v[96:99]\n"
"v_mfma_f32_16x16x32_bf16 v[100:103], v[180:183], v[44:47], v[100:103]\n"
"s_waitcnt vmcnt(10) lgkmcnt(2)\n"
"s_barrier\n"
"v_mfma_f32_16x16x32_bf16 v[56:59], v[184:187], v[32:35], v[56:59]\n"
"ds_read_b128 v[44:47], v7 offset:3136\n"
"ds_read_b64_tr_b16 v[208:209], v6 offset:0\n"
"v_mfma_f32_16x16x32_bf16 v[60:63], v[188:191], v[32:35], v[60:63]\n"
"s_mov_b32 m0, s68\n"
"v_mfma_f32_16x16x32_bf16 v[48:51], v[192:195], v[32:35], v[48:51]\n"
"buffer_load_dwordx4 v2, s[36:39], s79 offen nt lds\n"
"v_mfma_f32_16x16x32_bf16 v[52:55], v[196:199], v[32:35], v[52:55]\n"
"ds_read_b64_tr_b16 v[210:211], v6 offset:1024\n"
"s_waitcnt lgkmcnt(4)\n"
"v_mfma_f32_16x16x32_bf16 v[64:67], v[184:187], v[36:39], v[64:67]\n"
"ds_read_b128 v[32:35], v7 offset:4160\n"
"v_mfma_f32_16x16x32_bf16 v[68:71], v[188:191], v[36:39], v[68:71]\n"
"v_mfma_f32_16x16x32_bf16 v[56:59], v[192:195], v[36:39], v[56:59]\n"
"ds_read_b64_tr_b16 v[212:213], v6 offset:8\n"
"v_mfma_f32_16x16x32_bf16 v[60:63], v[196:199], v[36:39], v[60:63]\n"
"v_mfma_f32_16x16x32_bf16 v[48:51], v[200:203], v[36:39], v[48:51]\n"
"s_add_u32 m0, s68, 8256\n"
"v_mfma_f32_16x16x32_bf16 v[52:55], v[204:207], v[36:39], v[52:55]\n"
"ds_read_b64_tr_b16 v[214:215], v6 offset:1032\n"
"s_waitcnt lgkmcnt(6)\n"
"v_mfma_f32_16x16x32_bf16 v[72:75], v[184:187], v[40:43], v[72:75]\n"
"ds_read_b128 v[36:39], v7 offset:5184\n"
"v_mfma_f32_16x16x32_bf16 v[76:79], v[188:191], v[40:43], v[76:79]\n"
"buffer_load_dwordx4 v2, s[40:43], s79 offen nt lds\n"
"v_mfma_f32_16x16x32_bf16 v[64:67], v[192:195], v[40:43], v[64:67]\n"
"ds_read_b64_tr_b16 v[216:217], v6 offset:8192\n"
"v_mfma_f32_16x16x32_bf16 v[68:71], v[196:199], v[40:43], v[68:71]\n"
"v_mfma_f32_16x16x32_bf16 v[56:59], v[200:203], v[40:43], v[56:59]\n"
"v_mfma_f32_16x16x32_bf16 v[60:63], v[204:207], v[40:43], v[60:63]\n"
"ds_read_b64_tr_b16 v[218:219], v6 offset:9216\n"
"s_waitcnt lgkmcnt(8)\n"
"v_mfma_f32_16x16x32_bf16 v[80:83], v[184:187], v[44:47], v[80:83]\n"
"ds_read_b128 v[40:43], v7 offset:6208\n"
"s_add_u32 s79, s79, 1024\n"
"v_mfma_f32_16x16x32_bf16 v[84:87], v[188:191], v[44:47], v[84:87]\n"
"v_mfma_f32_16x16x32_bf16 v[72:75], v[192:195], v[44:47], v[72:75]\n"
"ds_read_b64_tr_b16 v[220:221], v6 offset:8200\n"
"s_add_u32 m0, s68, 1024\n"
"v_mfma_f32_16x16x32_bf16 v[76:79], v[196:199], v[44:47], v[76:79]\n"
"v_mfma_f32_16x16x32_bf16 v[64:67], v[200:203], v[44:47], v[64:67]\n"
"buffer_load_dwordx4 v2, s[36:39], s80 offen nt lds\n"
"v_mfma_f32_16x16x32_bf16 v[68:71], v[204:207], v[44:47], v[68:71]\n"
"ds_read_b64_tr_b16 v[222:223], v6 offset:9224\n"
"s_waitcnt lgkmcnt(8)\n"
"v_mfma_f32_16x16x32_bf16 v[88:91], v[184:187], v[32:35], v[88:91]\n"
"ds_read_b128 v[44:47], v7 offset:7232\n"
"v_mfma_f32_16x16x32_bf16 v[92:95], v[188:191], v[32:35], v[92:95]\n"
"v_mfma_f32_16x16x32_bf16 v[80:83], v[192:195], v[32:35], v[80:83]\n"
"ds_read_b64_tr_b16 v[224:225], v6 offset:16384\n"
"v_mfma_f32_16x16x32_bf16 v[84:87], v[196:199], v[32:35], v[84:87]\n"
"v_mfma_f32_16x16x32_bf16 v[72:75], v[200:203], v[32:35], v[72:75]\n"
"s_add_u32 m0, s68, 9280\n"
"v_mfma_f32_16x16x32_bf16 v[76:79], v[204:207], v[32:35], v[76:79]\n"
"ds_read_b64_tr_b16 v[226:227], v6 offset:17408\n"
"s_waitcnt lgkmcnt(8)\n"
"v_mfma_f32_16x16x32_bf16 v[96:99], v[184:187], v[36:39], v[96:99]\n"
"ds_read_b128 v[32:35], v7 offset:16448\n"
"v_mfma_f32_16x16x32_bf16 v[100:103], v[188:191], v[36:39], v[100:103]\n"
"buffer_load_dwordx4 v2, s[40:43], s80 offen nt lds\n"
"v_mfma_f32_16x16x32_bf16 v[88:91], v[192:195], v[36:39], v[88:91]\n"
"ds_read_b64_tr_b16 v[228:229], v6 offset:16392\n"
"v_mfma_f32_16x16x32_bf16 v[92:95], v[196:199], v[36:39], v[92:95]\n"
"v_mfma_f32_16x16x32_bf16 v[80:83], v[200:203], v[36:39], v[80:83]\n"
"v_mfma_f32_16x16x32_bf16 v[84:87], v[204:207], v[36:39], v[84:87]\n"
"ds_read_b64_tr_b16 v[230:231], v6 offset:17416\n"
"s_waitcnt lgkmcnt(8)\n"
"v_mfma_f32_16x16x32_bf16 v[104:107], v[184:187], v[40:43], v[104:107]\n"
"ds_read_b128 v[36:39], v7 offset:17472\n"
"s_add_u32 s80, s80, 1024\n"
"v_mfma_f32_16x16x32_bf16 v[108:111], v[188:191], v[40:43], v[108:111]\n"
"v_mfma_f32_16x16x32_bf16 v[96:99], v[192:195], v[40:43], v[96:99]\n"
"s_add_u32 s68, s68, 16448\n"
"v_mfma_f32_16x16x32_bf16 v[100:103], v[196:199], v[40:43], v[100:103]\n"
"v_mfma_f32_16x16x32_bf16 v[88:91], v[200:203], v[40:43], v[88:91]\n"
"s_cmp_eq_u32 s68, s69\ns_cselect_b32 s68, s81, s68\n"
"v_mfma_f32_16x16x32_bf16 v[92:95], v[204:207], v[40:43], v[92:95]\n"
"s_waitcnt lgkmcnt(6)\n"
"v_mfma_f32_16x16x32_bf16 v[112:115], v[184:187], v[44:47], v[112:115]\n"
"ds_read_b128 v[40:43], v7 offset:18496\n"
"v_mfma_f32_16x16x32_bf16 v[116:119], v[188:191], v[44:47], v[116:119]\n"
"v_mfma_f32_16x16x32_bf16 v[104:107], v[192:195], v[44:47], v[104:107]\n"
"v_mfma_f32_16x16x32_bf16 v[108:111], v[196:199], v[44:47], v[108:111]\n"
"v_mfma_f32_16x16x32_bf16 v[96:99], v[200:203], v[44:47], v[96:99]\n"
"v_mfma_f32_16x16x32_bf16 v[100:103], v[204:207], v[44:47], v[100:103]\n"
"s_waitcnt vmcnt(8) lgkmcnt(2)\n"
"s_barrier\n"
"v_mfma_f32_16x16x32_bf16 v[56:59], v[208:211], v[32:35], v[56:59]\n"
"ds_read_b128 v[44:47], v7 offset:19520\n"
"ds_read_b64_tr_b16 v[232:233], v6 offset:24576\n"
"v_mfma_f32_16x16x32_bf16 v[60:63], v[212:215], v[32:35], v[60:63]\n"
"s_mov_b32 m0, s68\n"
"v_mfma_f32_16x16x32_bf16 v[48:51], v[216:219], v[32:35], v[48:51]\n"
"buffer_load_dwordx4 v2, s[36:39], s79 offen nt lds\n"
"v_mfma_f32_16x16x32_bf16 v[52:55], v[220:223], v[32:35], v[52:55]\n"
"ds_read_b64_tr_b16 v[234:235], v6 offset:25600\n"
"s_waitcnt lgkmcnt(4)\n"
"v_mfma_f32_16x16x32_bf16 v[64:67], v[208:211], v[36:39], v[64:67]\n"
"ds_read_b128 v[32:35], v7 offset:20544\n"
"v_mfma_f32_16x16x32_bf16 v[68:71], v[212:215], v[36:39], v[68:71]\n"
"s_add_u32 m0, s68, 8256\n"
"v_mfma_f32_16x16x32_bf16 v[56:59], v[216:219], v[36:39], v[56:59]\n"
"ds_read_b64_tr_b16 v[236:237], v6 offset:24584\n"
"v_mfma_f32_16x16x32_bf16 v[60:63], v[220:223], v[36:39], v[60:63]\n"
"buffer_load_dwordx4 v2, s[40:43], s79 offen nt lds\n"
"v_mfma_f32_16x16x32_bf16 v[48:51], v[224:227], v[36:39], v[48:51]\n"
"s_add_u32 s79, s79, 1024\n"
"v_mfma_f32_16x16x32_bf16 v[52:55], v[228:231], v[36:39], v[52:55]\n"
"ds_read_b64_tr_b16 v[238:239], v6 offset:25608\n"
"s_waitcnt lgkmcnt(6)\n"
"v_mfma_f32_16x16x32_bf16 v[72:75], v[208:211], v[40:43], v[72:75]\n"
"ds_read_b128 v[36:39], v7 offset:21568\n"
"s_add_u32 m0, s68, 1024\n"
"v_mfma_f32_16x16x32_bf16 v[76:79], v[212:215], v[40:43], v[76:79]\n"
"buffer_load_dwordx4 v2, s[36:39], s80 offen nt lds\n"
"v_mfma_f32_16x16x32_bf16 v[64:67], v[216:219], v[40:43], v[64:67]\n"
"ds_read_b64_tr_b16 v[240:241], v6 offset:32768\n"
"v_mfma_f32_16x16x32_bf16 v[68:71], v[220:223], v[40:43], v[68:71]\n"
"s_add_u32 m0, s68, 9280\n"
"v_mfma_f32_16x16x32_bf16 v[56:59], v[224:227], v[40:43], v[56:59]\n"
"buffer_load_dwordx4 v2, s[40:43], s80 offen nt lds\n"
"v_mfma_f32_16x16x32_bf16 v[60:63], v[228:231], v[40:43], v[60:63]\n"
"ds_read_b64_tr_b16 v[242:243], v6 offset:33792\n"
"s_waitcnt lgkmcnt(8)\n"
"v_mfma_f32_16x16x32_bf16 v[80:83], v[208:211], v[44:47], v[80:83]\n"
"ds_read_b128 v[40:43], v7 offset:22592\n"
"v_mfma_f32_16x16x32_bf16 v[84:87], v[212:215], v[44:47], v[84:87]\n"
"s_add_u32 s80, s80, 1024\n"
"v_mfma_f32_16x16x32_bf16 v[72:75], v[216:219], v[44:47], v[72:75]\n"
"ds_read_b64_tr_b16 v[244:245], v6 offset:32776\n"
"v_mfma_f32_16x16x32_bf16 v[76:79], v[220:223], v[44:47], v[76:79]\n"
"s_add_u32 s68, s68, 16448\n"
"v_mfma_f32_16x16x32_bf16 v[64:67], v[224:227], v[44:47], v[64:67]\n"
"s_cmp_eq_u32 s68, s69\ns_cselect_b32 s68, s81, s68\n"
"v_mfma_f32_16x16x32_bf16 v[68:71], v[228:231], v[44:47], v[68:71]\n"
"ds_read_b64_tr_b16 v[246:247], v6 offset:33800\n"
"s_waitcnt lgkmcnt(8)\n"
"v_mfma_f32_16x16x32_bf16 v[88:91], v[208:211], v[32:35], v[88:91]\n"
"ds_read_b128 v[44:47], v7 offset:23616\n"
"v_mfma_f32_16x16x32_bf16 v[92:95], v[212:215], v[32:35], v[92:95]\n"
"s_mov_b32 m0, s68\n"
"v_mfma_f32_16x16x32_bf16 v[80:83], v[216:219], v[32:35], v[80:83]\n"
"ds_read_b64_tr_b16 v[248:249], v6 offset:40960\n"
"v_mfma_f32_16x16x32_bf16 v[84:87], v[220:223], v[32:35], v[84:87]\n"
"buffer_load_dwordx4 v2, s[36:39], s79 offen nt lds\n"
"v_mfma_f32_16x16x32_bf16 v[72:75], v[224:227], v[32:35], v[72:75]\n"
"s_add_u32 m0, s68, 8256\n"
"v_mfma_f32_16x16x32_bf16 v[76:79], v[228:231], v[32:35], v[76:79]\n"
"ds_read_b64_tr_b16 v[250:251], v6 offset:41984\n"
"s_waitcnt lgkmcnt(8)\n"
"v_mfma_f32_16x16x32_bf16 v[96:99], v[208:211], v[36:39], v[96:99]\n"
"ds_read_b128 v[32:35], v7 offset:16512\n"
"v_mfma_f32_16x16x32_bf16 v[100:103], v[212:215], v[36:39], v[100:103]\n"
"buffer_load_dwordx4 v2, s[40:43], s79 offen nt lds\n"
"v_mfma_f32_16x16x32_bf16 v[88:91], v[216:219], v[36:39], v[88:91]\n"
"ds_read_b64_tr_b16 v[252:253], v6 offset:40968\n"
"s_add_u32 s79, s79, 1024\n"
"v_mfma_f32_16x16x32_bf16 v[92:95], v[220:223], v[36:39], v[92:95]\n"
"s_add_u32 m0, s68, 1024\n"
"v_mfma_f32_16x16x32_bf16 v[80:83], v[224:227], v[36:39], v[80:83]\n"
"buffer_load_dwordx4 v2, s[36:39], s80 offen nt lds\n"
"v_mfma_f32_16x16x32_bf16 v[84:87], v[228:231], v[36:39], v[84:87]\n"
"ds_read_b64_tr_b16 v[254:255], v6 offset:41992\n"
"s_waitcnt lgkmcnt(8)\n"
"v_mfma_f32_16x16x32_bf16 v[104:107], v[208:211], v[40:43], v[104:107]\n"
"ds_read_b128 v[36:39], v7 offset:17536\n"
"v_mfma_f32_16x16x32_bf16 v[108:111], v[212:215], v[40:43], v[108:111]\n"
"s_add_u32 m0, s68, 9280\n"
"v_mfma_f32_16x16x32_bf16 v[96:99], v[216:219], v[40:43], v[96:99]\n"
"buffer_load_dwordx4 v2, s[40:43], s80 offen nt lds\n"
"v_mfma_f32_16x16x32_bf16 v[100:103], v[220:223], v[40:43], v[100:103]\n"
"v_mfma_f32_16x16x32_bf16 v[88:91], v[224:227], v[40:43], v[88:91]\n"
"s_add_u32 s80, s80, 1024\n"
"v_mfma_f32_16x16x32_bf16 v[92:95], v[228:231], v[40:43], v[92:95]\n"
"s_add_u32 s68, s68, 16448\n"
"s_waitcnt lgkmcnt(6)\n"
"v_mfma_f32_16x16x32_bf16 v[112:115], v[208:211], v[44:47], v[112:115]\n"
"ds_read_b128 v[40:43], v7 offset:18560\n"
"v_mfma_f32_16x16x32_bf16 v[116:119], v[212:215], v[44:47], v[116:119]\n"
"s_cmp_eq_u32 s68, s69\ns_cselect_b32 s68, s81, s68\n"
"v_mfma_f32_16x16x32_bf16 v[104:107], v[216:219], v[44:47], v[104:107]\n"
"v_mfma_f32_16x16x32_bf16 v[108:111], v[220:223], v[44:47], v[108:111]\n"
"v_mfma_f32_16x16x32_bf16 v[96:99], v[224:227], v[44:47], v[96:99]\n"
"v_mfma_f32_16x16x32_bf16 v[100:103], v[228:231], v[44:47], v[100:103]\n"
"s_waitcnt lgkmcnt(4)\n"
"v_mfma_f32_16x16x32_bf16 v[56:59], v[232:235], v[32:35], v[56:59]\n"
"ds_read_b128 v[44:47], v7 offset:19584\n"
"v_mfma_f32_16x16x32_bf16 v[60:63], v[236:239], v[32:35], v[60:63]\n"
"v_mfma_f32_16x16x32_bf16 v[48:51], v[240:243], v[32:35], v[48:51]\n"
"v_mfma_f32_16x16x32_bf16 v[52:55], v[244:247], v[32:35], v[52:55]\n"
"s_waitcnt lgkmcnt(2)\n"
"v_mfma_f32_16x16x32_bf16 v[64:67], v[232:235], v[36:39], v[64:67]\n"
"ds_read_b128 v[32:35], v7 offset:20608\n"
"v_mfma_f32_16x16x32_bf16 v[68:71], v[236:239], v[36:39], v[68:71]\n"
"v_mfma_f32_16x16x32_bf16 v[56:59], v[240:243], v[36:39], v[56:59]\n"
"v_mfma_f32_16x16x32_bf16 v[60:63], v[244:247], v[36:39], v[60:63]\n"
"v_mfma_f32_16x16x32_bf16 v[48:51], v[248:251], v[36:39], v[48:51]\n"
"v_mfma_f32_16x16x32_bf16 v[52:55], v[252:255], v[36:39], v[52:55]\n"
"s_waitcnt lgkmcnt(2)\n"
"v_mfma_f32_16x16x32_bf16 v[72:75], v[232:235], v[40:43], v[72:75]\n"
"ds_read_b128 v[36:39], v7 offset:21632\n"
"v_mfma_f32_16x16x32_bf16 v[76:79], v[236:239], v[40:43], v[76:79]\n"
"v_mfma_f32_16x16x32_bf16 v[64:67], v[240:243], v[40:43], v[64:67]\n"
"v_mfma_f32_16x16x32_bf16 v[68:71], v[244:247], v[40:43], v[68:71]\n"
"v_mfma_f32_16x16x32_bf16 v[56:59], v[248:251], v[40:43], v[56:59]\n"
"v_mfma_f32_16x16x32_bf16 v[60:63], v[252:255], v[40:43], v[60:63]\n"
"s_waitcnt lgkmcnt(2)\n"
"v_mfma_f32_16x16x32_bf16 v[80:83], v[232:235], v[44:47], v[80:83]\n"
"ds_read_b128 v[40:43], v7 offset:22656\n"
"v_fma_f32 v48, v48, v8, v16\n"
"v_mfma_f32_16x16x32_bf16 v[84:87], v[236:239], v[44:47], v[84:87]\n"
"v_fma_f32 v49, v49, v9, v17\n"
"v_fma_f32 v50, v50, v10, v18\n"
"v_mfma_f32_16x16x32_bf16 v[72:75], v[240:243], v[44:47], v[72:75]\n"
"v_fma_f32 v51, v51, v11, v19\n"
"v_mfma_f32_16x16x32_bf16 v[76:79], v[244:247], v[44:47], v[76:79]\n"
"v_fma_f32 v52, v52, v12, v20\n"
"v_fma_f32 v53, v53, v13, v21\n"
"v_mfma_f32_16x16x32_bf16 v[64:67], v[248:251], v[44:47], v[64:67]\n"
"v_fma_f32 v54, v54, v14, v22\n"
"v_fma_f32 v55, v55, v15, v23\n"
"v_mfma_f32_16x16x32_bf16 v[68:71], v[252:255], v[44:47], v[68:71]\n"
"s_waitcnt lgkmcnt(2)\n"
"v_mfma_f32_16x16x32_bf16 v[88:91], v[232:235], v[32:35], v[88:91]\n"
"ds_read_b128 v[44:47], v7 offset:23680\n"
"v_mfma_f32_16x16x32_bf16 v[92:95], v[236:239], v[32:35], v[92:95]\n"
"v_exp_f32 v24, v48\n"
"v_mfma_f32_16x16x32_bf16 v[80:83], v[240:243], v[32:35], v[80:83]\n"
"v_exp_f32 v25, v49\n"
"v_mfma_f32_16x16x32_bf16 v[84:87], v[244:247], v[32:35], v[84:87]\n"
"v_exp_f32 v26, v50\n"
"v_mfma_f32_16x16x32_bf16 v[72:75], v[248:251], v[32:35], v[72:75]\n"
"v_exp_f32 v27, v51\n"
"v_mfma_f32_16x16x32_bf16 v[76:79], v[252:255], v[32:35], v[76:79]\n"
"s_waitcnt vmcnt(12)\n"
"s_barrier\n"
"s_waitcnt lgkmcnt(2)\n"
"v_mfma_f32_16x16x32_bf16 v[96:99], v[232:235], v[36:39], v[96:99]\n"
"ds_read_b128 v[32:35], v0 offset:0\n"
"s_mov_b32 m0, s68\n"
"v_mfma_f32_16x16x32_bf16 v[100:103], v[236:239], v[36:39], v[100:103]\n"
"buffer_load_dwordx4 v2, s[36:39], s79 offen nt lds\n"
"v_mfma_f32_16x16x32_bf16 v[88:91], v[240:243], v[36:39], v[88:91]\n"
"s_add_u32 m0, s68, 8256\n"
"v_mfma_f32_16x16x32_bf16 v[92:95], v[244:247], v[36:39], v[92:95]\n"
"buffer_load_dwordx4 v2, s[40:43], s79 offen nt lds\n"
"v_mfma_f32_16x16x32_bf16 v[80:83], v[248:251], v[36:39], v[80:83]\n"
"v_exp_f32 v28, v52\n"
"v_mfma_f32_16x16x32_bf16 v[84:87], v[252:255], v[36:39], v[84:87]\n"
"s_add_u32 s79, s79, 1024\n"
"s_waitcnt lgkmcnt(2)\n"
"v_mfma_f32_16x16x32_bf16 v[104:107], v[232:235], v[40:43], v[104:107]\n"
"ds_read_b128 v[36:39], v0 offset:64\n"
"v_mfma_f32_16x16x32_bf16 v[108:111], v[236:239], v[40:43], v[108:111]\n"
"s_add_u32 m0, s68, 1024\n"
"v_mfma_f32_16x16x32_bf16 v[96:99], v[240:243], v[40:43], v[96:99]\n"
"buffer_load_dwordx4 v2, s[36:39], s80 offen nt lds\n"
"v_mfma_f32_16x16x32_bf16 v[100:103], v[244:247], v[40:43], v[100:103]\n"
"s_add_u32 m0, s68, 9280\n"
"v_mfma_f32_16x16x32_bf16 v[88:91], v[248:251], v[40:43], v[88:91]\n"
"buffer_load_dwordx4 v2, s[40:43], s80 offen nt lds\n"
"v_mfma_f32_16x16x32_bf16 v[92:95], v[252:255], v[40:43], v[92:95]\n"
"s_waitcnt lgkmcnt(2)\n"
"v_mfma_f32_16x16x32_bf16 v[112:115], v[232:235], v[44:47], v[112:115]\n"
"ds_read_b128 v[40:43], v0 offset:1024\n"
"v_mfma_f32_16x16x32_bf16 v[116:119], v[236:239], v[44:47], v[116:119]\n"
"s_add_u32 s80, s80, 1024\n"
"s_add_u32 s68, s68, 16448\n"
"v_mfma_f32_16x16x32_bf16 v[104:107], v[240:243], v[44:47], v[104:107]\n"
"s_cmp_eq_u32 s68, s69\ns_cselect_b32 s68, s81, s68\n"
"v_mfma_f32_16x16x32_bf16 v[108:111], v[244:247], v[44:47], v[108:111]\n"
"v_exp_f32 v29, v53\n"
"v_mfma_f32_16x16x32_bf16 v[96:99], v[248:251], v[44:47], v[96:99]\n"
"v_exp_f32 v30, v54\n"
"v_mfma_f32_16x16x32_bf16 v[100:103], v[252:255], v[44:47], v[100:103]\n"
"s_waitcnt lgkmcnt(2)\n"
"v_mfma_f32_16x16x32_bf16 v[120:123], a[0:3], v[32:35], 0\n"
"ds_read_b128 v[44:47], v0 offset:1088\n"
"v_mfma_f32_16x16x32_bf16 v[124:127], a[4:7], v[32:35], 0\n"
"s_add_u32 s70, s70, 16448\n"
"v_mfma_f32_16x16x32_bf16 v[112:115], a[8:11], v[32:35], v[112:115]\n"
"v_exp_f32 v31, v55\n"
"v_mfma_f32_16x16x32_bf16 v[116:119], a[12:15], v[32:35], v[116:119]\n"
"s_cmp_eq_u32 s70, 148032\ns_cselect_b32 s70, 0, s70\n"
"v_mfma_f32_16x16x32_bf16 v[104:107], a[16:19], v[32:35], v[104:107]\n"
"v_fma_f32 v24, v24, s83, s83\n"
"v_fma_f32 v25, v25, s83, s83\n"
"v_mfma_f32_16x16x32_bf16 v[108:111], a[20:23], v[32:35], v[108:111]\n"
"v_fma_f32 v26, v26, s83, s83\n"
"s_waitcnt lgkmcnt(2)\n"
"v_mfma_f32_16x16x32_bf16 v[120:123], a[24:27], v[36:39], v[120:123]\n"
"ds_read_b128 v[32:35], v0 offset:2048\n"
"v_fma_f32 v27, v27, s83, s83\n"
"v_mfma_f32_16x16x32_bf16 v[124:127], a[28:31], v[36:39], v[124:127]\n"
"v_fma_f32 v28, v28, s83, s83\n"
"v_fma_f32 v29, v29, s83, s83\n"
"v_mfma_f32_16x16x32_bf16 v[112:115], a[32:35], v[36:39], v[112:115]\n"
"s_mov_b32 m0, s68\n"
"v_fma_f32 v30, v30, s83, s83\n"
"v_mfma_f32_16x16x32_bf16 v[116:119], a[36:39], v[36:39], v[116:119]\n"
"v_fma_f32 v31, v31, s83, s83\n"
"v_mfma_f32_16x16x32_bf16 v[104:107], a[40:43], v[36:39], v[104:107]\n"
"v_rcp_f32 v24, v24\n"
"v_mfma_f32_16x16x32_bf16 v[108:111], a[44:47], v[36:39], v[108:111]\n"
"s_waitcnt lgkmcnt(2)\n"
"v_mfma_f32_16x16x32_bf16 v[120:123], a[48:51], v[40:43], v[120:123]\n"
"ds_read_b128 v[36:39], v0 offset:2112\n"
"v_mfma_f32_16x16x32_bf16 v[124:127], a[52:55], v[40:43], v[124:127]\n"
"buffer_load_dwordx4 v2, s[36:39], s79 offen nt lds\n"
"v_mfma_f32_16x16x32_bf16 v[112:115], a[56:59], v[40:43], v[112:115]\n"
"v_rcp_f32 v25, v25\n"
"v_mfma_f32_16x16x32_bf16 v[116:119], a[60:63], v[40:43], v[116:119]\n"
"v_rcp_f32 v26, v26\n"
"v_mfma_f32_16x16x32_bf16 v[104:107], a[64:67], v[40:43], v[104:107]\n"
"s_add_u32 m0, s68, 8256\n"
"v_mfma_f32_16x16x32_bf16 v[108:111], a[68:71], v[40:43], v[108:111]\n"
"s_waitcnt lgkmcnt(2)\n"
"v_mfma_f32_16x16x32_bf16 v[120:123], a[72:75], v[44:47], v[120:123]\n"
"ds_read_b128 v[40:43], v0 offset:3072\n"
"v_mfma_f32_16x16x32_bf16 v[124:127], a[76:79], v[44:47], v[124:127]\n"
"buffer_load_dwordx4 v2, s[40:43], s79 offen nt lds\n"
"v_mfma_f32_16x16x32_bf16 v[112:115], a[80:83], v[44:47], v[112:115]\n"
"v_rcp_f32 v27, v27\n"
"v_mfma_f32_16x16x32_bf16 v[116:119], a[84:87], v[44:47], v[116:119]\n"
"v_rcp_f32 v28, v28\n"
"v_mfma_f32_16x16x32_bf16 v[104:107], a[88:91], v[44:47], v[104:107]\n"
"v_rcp_f32 v29, v29\n"
"v_mfma_f32_16x16x32_bf16 v[108:111], a[92:95], v[44:47], v[108:111]\n"
"s_waitcnt lgkmcnt(2)\n"
"v_mfma_f32_16x16x32_bf16 v[120:123], a[96:99], v[32:35], v[120:123]\n"
"ds_read_b128 v[44:47], v0 offset:3136\n"
"v_mfma_f32_16x16x32_bf16 v[124:127], a[100:103], v[32:35], v[124:127]\n"
"s_add_u32 s79, s79, 1024\n"
"v_mfma_f32_16x16x32_bf16 v[112:115], a[104:107], v[32:35], v[112:115]\n"
"v_rcp_f32 v30, v30\n"
"v_mfma_f32_16x16x32_bf16 v[116:119], a[108:111], v[32:35], v[116:119]\n"
"s_add_u32 m0, s68, 1024\n"
"v_mfma_f32_16x16x32_bf16 v[104:107], a[112:115], v[32:35], v[104:107]\n"
"v_rcp_f32 v31, v31\n"
"v_mfma_f32_16x16x32_bf16 v[108:111], a[116:119], v[32:35], v[108:111]\n"
"v_mul_f32 v48, v48, v24\n"
"s_waitcnt lgkmcnt(2)\n"
"v_mfma_f32_16x16x32_bf16 v[120:123], a[120:123], v[36:39], v[120:123]\n"
"ds_read_b128 v[32:35], v0 offset:4096\n"
"v_mul_f32 v49, v49, v25\n"
"v_mfma_f32_16x16x32_bf16 v[124:127], a[124:127], v[36:39], v[124:127]\n"
"buffer_load_dwordx4 v2, s[36:39], s80 offen nt lds\n"
"v_mfma_f32_16x16x32_bf16 v[112:115], a[128:131], v[36:39], v[112:115]\n"
"v_mul_f32 v50, v50, v26\n"
"v_mul_f32 v51, v51, v27\n"
"v_mfma_f32_16x16x32_bf16 v[116:119], a[132:135], v[36:39], v[116:119]\n"
"v_mul_f32 v52, v52, v28\n"
"v_mul_f32 v53, v53, v29\n"
"v_mfma_f32_16x16x32_bf16 v[104:107], a[136:139], v[36:39], v[104:107]\n"
"v_mul_f32 v54, v54, v30\n"
"v_mul_f32 v55, v55, v31\n"
"v_cvt_pk_bf16_f32 v48, v48, v49\n"
"v_cvt_pk_bf16_f32 v49, v50, v51\n"
"v_cvt_pk_bf16_f32 v50, v52, v53\n"
"v_cvt_pk_bf16_f32 v51, v54, v55\n"
"buffer_store_dwordx4 v[48:51], v3, s[56:59], s82 offen offset:0 sc1 nt\n"
"v_mfma_f32_16x16x32_bf16 v[108:111], a[140:143], v[36:39], v[108:111]\n"
"v_fma_f32 v56, v56, v8, v16\n"
"s_waitcnt lgkmcnt(2)\n"
"v_mfma_f32_16x16x32_bf16 v[120:123], a[144:147], v[40:43], v[120:123]\n"
"ds_read_b128 v[36:39], v0 offset:4160\n"
"s_add_u32 m0, s68, 9280\n"
"v_mfma_f32_16x16x32_bf16 v[124:127], a[148:151], v[40:43], v[124:127]\n"
"v_fma_f32 v57, v57, v9, v17\n"
"v_fma_f32 v58, v58, v10, v18\n"
"v_mfma_f32_16x16x32_bf16 v[112:115], a[152:155], v[40:43], v[112:115]\n"
"buffer_load_dwordx4 v2, s[40:43], s80 offen nt lds\n"
"v_mfma_f32_16x16x32_bf16 v[116:119], a[156:159], v[40:43], v[116:119]\n"
"v_fma_f32 v59, v59, v11, v19\n"
"v_fma_f32 v60, v60, v12, v20\n"
"v_mfma_f32_16x16x32_bf16 v[104:107], a[160:163], v[40:43], v[104:107]\n"
"v_fma_f32 v61, v61, v13, v21\n"
"v_mfma_f32_16x16x32_bf16 v[108:111], a[164:167], v[40:43], v[108:111]\n"
"v_fma_f32 v62, v62, v14, v22\n"
"s_waitcnt lgkmcnt(2)\n"
"v_mfma_f32_16x16x32_bf16 v[120:123], a[168:171], v[44:47], v[120:123]\n"
"ds_read_b128 v[40:43], v0 offset:5120\n"
"v_fma_f32 v63, v63, v15, v23\n"
"v_mfma_f32_16x16x32_bf16 v[124:127], a[172:175], v[44:47], v[124:127]\n"
"v_exp_f32 v24, v56\n"
"v_mfma_f32_16x16x32_bf16 v[112:115], a[176:179], v[44:47], v[112:115]\n"
"s_add_u32 s80, s80, 1024\n"
"v_mfma_f32_16x16x32_bf16 v[116:119], a[180:183], v[44:47], v[116:119]\n"
"v_exp_f32 v25, v57\n"
"v_mfma_f32_16x16x32_bf16 v[104:107], a[184:187], v[44:47], v[104:107]\n"
"s_add_u32 s68, s68, 16448\n"
"v_mfma_f32_16x16x32_bf16 v[108:111], a[188:191], v[44:47], v[108:111]\n"
"s_waitcnt lgkmcnt(2)\n"
"v_mfma_f32_16x16x32_bf16 v[120:123], a[192:195], v[32:35], v[120:123]\n"
"ds_read_b128 v[44:47], v0 offset:5184\n"
"v_mfma_f32_16x16x32_bf16 v[124:127], a[196:199], v[32:35], v[124:127]\n"
"s_cmp_eq_u32 s68, s69\ns_cselect_b32 s68, s81, s68\n"
"v_mfma_f32_16x16x32_bf16 v[112:115], a[200:203], v[32:35], v[112:115]\n"
"v_exp_f32 v26, v58\n"
"v_mfma_f32_16x16x32_bf16 v[116:119], a[204:207], v[32:35], v[116:119]\n"
"v_exp_f32 v27, v59\n"
"v_mfma_f32_16x16x32_bf16 v[104:107], a[208:211], v[32:35], v[104:107]\n"
"v_exp_f32 v28, v60\n"
"v_mfma_f32_16x16x32_bf16 v[108:111], a[212:215], v[32:35], v[108:111]\n"
"s_waitcnt lgkmcnt(2)\n"
"v_mfma_f32_16x16x32_bf16 v[120:123], a[216:219], v[36:39], v[120:123]\n"
"ds_read_b128 v[32:35], v0 offset:6144\n"
"v_mfma_f32_16x16x32_bf16 v[124:127], a[220:223], v[36:39], v[124:127]\n"
"v_exp_f32 v29, v61\n"
"v_mfma_f32_16x16x32_bf16 v[112:115], a[224:227], v[36:39], v[112:115]\n"
"v_exp_f32 v30, v62\n"
"v_mfma_f32_16x16x32_bf16 v[116:119], a[228:231], v[36:39], v[116:119]\n"
"v_exp_f32 v31, v63\n"
"v_mfma_f32_16x16x32_bf16 v[104:107], a[232:235], v[36:39], v[104:107]\n"
"v_fma_f32 v24, v24, s83, s83\n"
"v_fma_f32 v25, v25, s83, s83\n"
"v_mfma_f32_16x16x32_bf16 v[108:111], a[236:239], v[36:39], v[108:111]\n"
"v_fma_f32 v26, v26, s83, s83\n"
"s_waitcnt lgkmcnt(2)\n"
"v_mfma_f32_16x16x32_bf16 v[120:123], a[240:243], v[40:43], v[120:123]\n"
"ds_read_b128 v[36:39], v0 offset:6208\n"
"v_fma_f32 v27, v27, s83, s83\n"
"v_mfma_f32_16x16x32_bf16 v[124:127], a[244:247], v[40:43], v[124:127]\n"
"v_fma_f32 v28, v28, s83, s83\n"
"v_fma_f32 v29, v29, s83, s83\n"
"v_mfma_f32_16x16x32_bf16 v[112:115], a[248:251], v[40:43], v[112:115]\n"
"v_fma_f32 v30, v30, s83, s83\n"
"v_fma_f32 v31, v31, s83, s83\n"
"v_mfma_f32_16x16x32_bf16 v[116:119], a[252:255], v[40:43], v[116:119]\n"
"v_rcp_f32 v24, v24\n"
"v_mfma_f32_16x16x32_bf16 v[104:107], v[128:131], v[40:43], v[104:107]\n"
"v_rcp_f32 v25, v25\n"
"v_mfma_f32_16x16x32_bf16 v[108:111], v[132:135], v[40:43], v[108:111]\n"
"s_waitcnt lgkmcnt(2)\n"
"v_mfma_f32_16x16x32_bf16 v[120:123], v[136:139], v[44:47], v[120:123]\n"
"ds_read_b128 v[40:43], v0 offset:7168\n"
"v_mfma_f32_16x16x32_bf16 v[124:127], v[140:143], v[44:47], v[124:127]\n"
"v_rcp_f32 v26, v26\n"
"v_mfma_f32_16x16x32_bf16 v[112:115], v[144:147], v[44:47], v[112:115]\n"
"v_rcp_f32 v27, v27\n"
"v_mfma_f32_16x16x32_bf16 v[116:119], v[148:151], v[44:47], v[116:119]\n"
"v_rcp_f32 v28, v28\n"
"v_mfma_f32_16x16x32_bf16 v[104:107], v[152:155], v[44:47], v[104:107]\n"
"v_rcp_f32 v29, v29\n"
"v_mfma_f32_16x16x32_bf16 v[108:111], v[156:159], v[44:47], v[108:111]\n"
"s_waitcnt vmcnt(12)\n"
"s_barrier\n"
"s_waitcnt lgkmcnt(2)\n"
"v_mfma_f32_16x16x32_bf16 v[120:123], v[160:163], v[32:35], v[120:123]\n"
"ds_read_b128 v[44:47], v0 offset:7232\n"
"v_mfma_f32_16x16x32_bf16 v[124:127], v[164:167], v[32:35], v[124:127]\n"
"v_add_u32 v0, s70, v1\n"
"v_mfma_f32_16x16x32_bf16 v[112:115], v[168:171], v[32:35], v[112:115]\n"
"v_rcp_f32 v30, v30\n"
"v_mfma_f32_16x16x32_bf16 v[116:119], v[172:175], v[32:35], v[116:119]\n"
"v_rcp_f32 v31, v31\n"
"v_mfma_f32_16x16x32_bf16 v[104:107], v[176:179], v[32:35], v[104:107]\n"
"v_mul_f32 v56, v56, v24\n"
"v_mul_f32 v57, v57, v25\n"
"v_mfma_f32_16x16x32_bf16 v[108:111], v[180:183], v[32:35], v[108:111]\n"
"s_waitcnt lgkmcnt(2)\n"
"v_mfma_f32_16x16x32_bf16 v[120:123], v[184:187], v[36:39], v[120:123]\n"
"ds_read_b128 v[32:35], v0 offset:0\n"
"v_mul_f32 v58, v58, v26\n"
"v_mfma_f32_16x16x32_bf16 v[124:127], v[188:191], v[36:39], v[124:127]\n"
"v_mul_f32 v59, v59, v27\n"
"v_mul_f32 v60, v60, v28\n"
"v_mfma_f32_16x16x32_bf16 v[112:115], v[192:195], v[36:39], v[112:115]\n"
"v_mul_f32 v61, v61, v29\n"
"v_mfma_f32_16x16x32_bf16 v[116:119], v[196:199], v[36:39], v[116:119]\n"
"v_mul_f32 v62, v62, v30\n"
"v_mul_f32 v63, v63, v31\n"
"v_mfma_f32_16x16x32_bf16 v[104:107], v[200:203], v[36:39], v[104:107]\n"
"v_cvt_pk_bf16_f32 v56, v56, v57\n"
"v_cvt_pk_bf16_f32 v57, v58, v59\n"
"v_mfma_f32_16x16x32_bf16 v[108:111], v[204:207], v[36:39], v[108:111]\n"
"s_waitcnt lgkmcnt(2)\n"
"v_mfma_f32_16x16x32_bf16 v[120:123], v[208:211], v[40:43], v[120:123]\n"
"ds_read_b128 v[36:39], v0 offset:64\n"
"v_cvt_pk_bf16_f32 v58, v60, v61\n"
"v_mfma_f32_16x16x32_bf16 v[124:127], v[212:215], v[40:43], v[124:127]\n"
"v_cvt_pk_bf16_f32 v59, v62, v63\n"
"v_mfma_f32_16x16x32_bf16 v[112:115], v[216:219], v[40:43], v[112:115]\n"
"buffer_store_dwordx4 v[56:59], v3, s[56:59], s82 offen offset:1024 sc1 nt\n"
"v_mfma_f32_16x16x32_bf16 v[116:119], v[220:223], v[40:43], v[116:119]\n"
"v_fma_f32 v64, v64, v8, v16\n"
"v_fma_f32 v65, v65, v9, v17\n"
"v_mfma_f32_16x16x32_bf16 v[104:107], v[224:227], v[40:43], v[104:107]\n"
"v_fma_f32 v66, v66, v10, v18\n"
"v_fma_f32 v67, v67, v11, v19\n"
"v_mfma_f32_16x16x32_bf16 v[108:111], v[228:231], v[40:43], v[108:111]\n"
"v_fma_f32 v68, v68, v12, v20\n"
"s_waitcnt lgkmcnt(2)\n"
"v_mfma_f32_16x16x32_bf16 v[120:123], v[232:235], v[44:47], v[120:123]\n"
"ds_read_b128 v[40:43], v0 offset:1024\n"
"v_mfma_f32_16x16x32_bf16 v[124:127], v[236:239], v[44:47], v[124:127]\n"
"v_fma_f32 v69, v69, v13, v21\n"
"v_fma_f32 v70, v70, v14, v22\n"
"v_mfma_f32_16x16x32_bf16 v[112:115], v[240:243], v[44:47], v[112:115]\n"
"v_fma_f32 v71, v71, v15, v23\n"
"v_mfma_f32_16x16x32_bf16 v[116:119], v[244:247], v[44:47], v[116:119]\n"
"v_exp_f32 v24, v64\n"
"v_mfma_f32_16x16x32_bf16 v[104:107], v[248:251], v[44:47], v[104:107]\n"
"v_exp_f32 v25, v65\n"
"v_mfma_f32_16x16x32_bf16 v[108:111], v[252:255], v[44:47], v[108:111]\n"
"s_waitcnt lgkmcnt(2)\n"
"v_mfma_f32_16x16x32_bf16 v[48:51], a[0:3], v[32:35], 0\n"
"ds_read_b128 v[44:47], v0 offset:1088\n"
"v_mfma_f32_16x16x32_bf16 v[52:55], a[4:7], v[32:35], 0\n"
"s_add_u32 s70, s70, 16448\n"
"v_mfma_f32_16x16x32_bf16 v[120:123], a[8:11], v[32:35], v[120:123]\n"
"v_exp_f32 v26, v66\n"
"v_mfma_f32_16x16x32_bf16 v[124:127], a[12:15], v[32:35], v[124:127]\n"
"s_cmp_eq_u32 s70, 148032\ns_cselect_b32 s70, 0, s70\n"
"v_mfma_f32_16x16x32_bf16 v[112:115], a[16:19], v[32:35], v[112:115]\n"
"v_exp_f32 v27, v67\n"
"v_mfma_f32_16x16x32_bf16 v[116:119], a[20:23], v[32:35], v[116:119]\n"
"s_waitcnt lgkmcnt(2)\n"
"v_mfma_f32_16x16x32_bf16 v[48:51], a[24:27], v[36:39], v[48:51]\n"
"ds_read_b128 v[32:35], v0 offset:2048\n"
"v_mfma_f32_16x16x32_bf16 v[52:55], a[28:31], v[36:39], v[52:55]\n"
"v_exp_f32 v28, v68\n"
"v_mfma_f32_16x16x32_bf16 v[120:123], a[32:35], v[36:39], v[120:123]\n"
"s_mov_b32 m0, s68\n"
"v_mfma_f32_16x16x32_bf16 v[124:127], a[36:39], v[36:39], v[124:127]\n"
"v_exp_f32 v29, v69\n"
"v_mfma_f32_16x16x32_bf16 v[112:115], a[40:43], v[36:39], v[112:115]\n"
"v_exp_f32 v30, v70\n"
"v_mfma_f32_16x16x32_bf16 v[116:119], a[44:47], v[36:39], v[116:119]\n"
"s_waitcnt lgkmcnt(2)\n"
"v_mfma_f32_16x16x32_bf16 v[48:51], a[48:51], v[40:43], v[48:51]\n"
"ds_read_b128 v[36:39], v0 offset:2112\n"
"v_mfma_f32_16x16x32_bf16 v[52:55], a[52:55], v[40:43], v[52:55]\n"
"buffer_load_dwordx4 v2, s[36:39], s79 offen nt lds\n"
"v_mfma_f32_16x16x32_bf16 v[120:123], a[56:59], v[40:43], v[120:123]\n"
"v_exp_f32 v31, v71\n"
"v_mfma_f32_16x16x32_bf16 v[124:127], a[60:63], v[40:43], v[124:127]\n"
"v_fma_f32 v24, v24, s83, s83\n"
"v_fma_f32 v25, v25, s83, s83\n"
"v_mfma_f32_16x16x32_bf16 v[112:115], a[64:67], v[40:43], v[112:115]\n"
"s_add_u32 m0, s68, 8256\n"
"v_fma_f32 v26, v26, s83, s83\n"
"v_mfma_f32_16x16x32_bf16 v[116:119], a[68:71], v[40:43], v[116:119]\n"
"v_fma_f32 v27, v27, s83, s83\n"
"s_waitcnt lgkmcnt(2)\n"
"v_mfma_f32_16x16x32_bf16 v[48:51], a[72:75], v[44:47], v[48:51]\n"
"ds_read_b128 v[40:43], v0 offset:3072\n"
"v_fma_f32 v28, v28, s83, s83\n"
"v_mfma_f32_16x16x32_bf16 v[52:55], a[76:79], v[44:47], v[52:55]\n"
"buffer_load_dwordx4 v2, s[40:43], s79 offen nt lds\n"
"v_mfma_f32_16x16x32_bf16 v[120:123], a[80:83], v[44:47], v[120:123]\n"
"v_fma_f32 v29, v29, s83, s83\n"
"v_fma_f32 v30, v30, s83, s83\n"
"v_mfma_f32_16x16x32_bf16 v[124:127], a[84:87], v[44:47], v[124:127]\n"
"v_fma_f32 v31, v31, s83, s83\n"
"v_mfma_f32_16x16x32_bf16 v[112:115], a[88:91], v[44:47], v[112:115]\n"
"v_rcp_f32 v24, v24\n"
"v_mfma_f32_16x16x32_bf16 v[116:119], a[92:95], v[44:47], v[116:119]\n"
"s_waitcnt lgkmcnt(2)\n"
"v_mfma_f32_16x16x32_bf16 v[48:51], a[96:99], v[32:35], v[48:51]\n"
"ds_read_b128 v[44:47], v0 offset:3136\n"
"v_mfma_f32_16x16x32_bf16 v[52:55], a[100:103], v[32:35], v[52:55]\n"
"s_add_u32 s79, s79, 1024\n"
"v_mfma_f32_16x16x32_bf16 v[120:123], a[104:107], v[32:35], v[120:123]\n"
"v_rcp_f32 v25, v25\n"
"v_mfma_f32_16x16x32_bf16 v[124:127], a[108:111], v[32:35], v[124:127]\n"
"s_add_u32 m0, s68, 1024\n"
"v_mfma_f32_16x16x32_bf16 v[112:115], a[112:115], v[32:35], v[112:115]\n"
"v_rcp_f32 v26, v26\n"
"v_mfma_f32_16x16x32_bf16 v[116:119], a[116:119], v[32:35], v[116:119]\n"
"s_waitcnt lgkmcnt(2)\n"
"v_mfma_f32_16x16x32_bf16 v[48:51], a[120:123], v[36:39], v[48:51]\n"
"ds_read_b128 v[32:35], v0 offset:4096\n"
"v_mfma_f32_16x16x32_bf16 v[52:55], a[124:127], v[36:39], v[52:55]\n"
"buffer_load_dwordx4 v2, s[36:39], s80 offen nt lds\n"
"v_mfma_f32_16x16x32_bf16 v[120:123], a[128:131], v[36:39], v[120:123]\n"
"v_rcp_f32 v27, v27\n"
"v_mfma_f32_16x16x32_bf16 v[124:127], a[132:135], v[36:39], v[124:127]\n"
"v_rcp_f32 v28, v28\n"
"v_mfma_f32_16x16x32_bf16 v[112:115], a[136:139], v[36:39], v[112:115]\n"
"v_rcp_f32 v29, v29\n"
"v_mfma_f32_16x16x32_bf16 v[116:119], a[140:143], v[36:39], v[116:119]\n"
"s_waitcnt lgkmcnt(2)\n"
"v_mfma_f32_16x16x32_bf16 v[48:51], a[144:147], v[40:43], v[48:51]\n"
"ds_read_b128 v[36:39], v0 offset:4160\n"
"s_add_u32 m0, s68, 9280\n"
"v_mfma_f32_16x16x32_bf16 v[52:55], a[148:151], v[40:43], v[52:55]\n"
"v_rcp_f32 v30, v30\n"
"v_mfma_f32_16x16x32_bf16 v[120:123], a[152:155], v[40:43], v[120:123]\n"
"buffer_load_dwordx4 v2, s[40:43], s80 offen nt lds\n"
"v_mfma_f32_16x16x32_bf16 v[124:127], a[156:159], v[40:43], v[124:127]\n"
"v_rcp_f32 v31, v31\n"
"v_mfma_f32_16x16x32_bf16 v[112:115], a[160:163], v[40:43], v[112:115]\n"
"v_mul_f32 v64, v64, v24\n"
"v_mul_f32 v65, v65, v25\n"
"v_mfma_f32_16x16x32_bf16 v[116:119], a[164:167], v[40:43], v[116:119]\n"
"v_mul_f32 v66, v66, v26\n"
"s_waitcnt lgkmcnt(2)\n"
"v_mfma_f32_16x16x32_bf16 v[48:51], a[168:171], v[44:47], v[48:51]\n"
"ds_read_b128 v[40:43], v0 offset:5120\n"
"v_mul_f32 v67, v67, v27\n"
"v_mfma_f32_16x16x32_bf16 v[52:55], a[172:175], v[44:47], v[52:55]\n"
"v_mul_f32 v68, v68, v28\n"
"v_mul_f32 v69, v69, v29\n"
"v_mfma_f32_16x16x32_bf16 v[120:123], a[176:179], v[44:47], v[120:123]\n"
"s_add_u32 s80, s80, 1024\n"
"v_mul_f32 v70, v70, v30\n"
"v_mfma_f32_16x16x32_bf16 v[124:127], a[180:183], v[44:47], v[124:127]\n"
"v_mul_f32 v71, v71, v31\n"
"v_cvt_pk_bf16_f32 v64, v64, v65\n"
"v_mfma_f32_16x16x32_bf16 v[112:115], a[184:187], v[44:47], v[112:115]\n"
"s_add_u32 s68, s68, 16448\n"
"v_cvt_pk_bf16_f32 v65, v66, v67\n"
"v_cvt_pk_bf16_f32 v66, v68, v69\n"
"v_cvt_pk_bf16_f32 v67, v70, v71\n"
"buffer_store_dwordx4 v[64:67], v3, s[56:59], s82 offen offset:2048 sc1 nt\n"
"v_mfma_f32_16x16x32_bf16 v[116:119], a[188:191], v[44:47], v[116:119]\n"
"v_fma_f32 v72, v72, v8, v16\n"
"s_waitcnt lgkmcnt(2)\n"
"v_mfma_f32_16x16x32_bf16 v[48:51], a[192:195], v[32:35], v[48:51]\n"
"ds_read_b128 v[44:47], v0 offset:5184\n"
"v_fma_f32 v73, v73, v9, v17\n"
"v_mfma_f32_16x16x32_bf16 v[52:55], a[196:199], v[32:35], v[52:55]\n"
"s_cmp_eq_u32 s68, s69\ns_cselect_b32 s68, s81, s68\n"
"v_mfma_f32_16x16x32_bf16 v[120:123], a[200:203], v[32:35], v[120:123]\n"
"v_fma_f32 v74, v74, v10, v18\n"
"v_fma_f32 v75, v75, v11, v19\n"
"v_mfma_f32_16x16x32_bf16 v[124:127], a[204:207], v[32:35], v[124:127]\n"
"v_fma_f32 v76, v76, v12, v20\n"
"v_mfma_f32_16x16x32_bf16 v[112:115], a[208:211], v[32:35], v[112:115]\n"
"v_fma_f32 v77, v77, v13, v21\n"
"v_fma_f32 v78, v78, v14, v22\n"
"v_mfma_f32_16x16x32_bf16 v[116:119], a[212:215], v[32:35], v[116:119]\n"
"v_fma_f32 v79, v79, v15, v23\n"
"s_waitcnt lgkmcnt(2)\n"
"v_mfma_f32_16x16x32_bf16 v[48:51], a[216:219], v[36:39], v[48:51]\n"
"ds_read_b128 v[32:35], v0 offset:6144\n"
"v_mfma_f32_16x16x32_bf16 v[52:55], a[220:223], v[36:39], v[52:55]\n"
"v_exp_f32 v24, v72\n"
"v_mfma_f32_16x16x32_bf16 v[120:123], a[224:227], v[36:39], v[120:123]\n"
"v_exp_f32 v25, v73\n"
"v_mfma_f32_16x16x32_bf16 v[124:127], a[228:231], v[36:39], v[124:127]\n"
"v_exp_f32 v26, v74\n"
"v_mfma_f32_16x16x32_bf16 v[112:115], a[232:235], v[36:39], v[112:115]\n"
"v_exp_f32 v27, v75\n"
"v_mfma_f32_16x16x32_bf16 v[116:119], a[236:239], v[36:39], v[116:119]\n"
"s_waitcnt lgkmcnt(2)\n"
"v_mfma_f32_16x16x32_bf16 v[48:51], a[240:243], v[40:43], v[48:51]\n"
"ds_read_b128 v[36:39], v0 offset:6208\n"
"v_mfma_f32_16x16x32_bf16 v[52:55], a[244:247], v[40:43], v[52:55]\n"
"v_exp_f32 v28, v76\n"
"v_mfma_f32_16x16x32_bf16 v[120:123], a[248:251], v[40:43], v[120:123]\n"
"v_exp_f32 v29, v77\n"
"v_mfma_f32_16x16x32_bf16 v[124:127], a[252:255], v[40:43], v[124:127]\n"
"v_exp_f32 v30, v78\n"
"v_mfma_f32_16x16x32_bf16 v[112:115], v[128:131], v[40:43], v[112:115]\n"
"v_exp_f32 v31, v79\n"
"v_mfma_f32_16x16x32_bf16 v[116:119], v[132:135], v[40:43], v[116:119]\n"
"s_waitcnt lgkmcnt(2)\n"
"v_mfma_f32_16x16x32_bf16 v[48:51], v[136:139], v[44:47], v[48:51]\n"
"ds_read_b128 v[40:43], v0 offset:7168\n"
"v_fma_f32 v24, v24, s83, s83\n"
"v_mfma_f32_16x16x32_bf16 v[52:55], v[140:143], v[44:47], v[52:55]\n"
"v_fma_f32 v25, v25, s83, s83\n"
"v_fma_f32 v26, v26, s83, s83\n"
"v_mfma_f32_16x16x32_bf16 v[120:123], v[144:147], v[44:47], v[120:123]\n"
"v_fma_f32 v27, v27, s83, s83\n"
"v_mfma_f32_16x16x32_bf16 v[124:127], v[148:151], v[44:47], v[124:127]\n"
"v_fma_f32 v28, v28, s83, s83\n"
"v_fma_f32 v29, v29, s83, s83\n"
"v_mfma_f32_16x16x32_bf16 v[112:115], v[152:155], v[44:47], v[112:115]\n"
"v_fma_f32 v30, v30, s83, s83\n"
"v_fma_f32 v31, v31, s83, s83\n"
"v_mfma_f32_16x16x32_bf16 v[116:119], v[156:159], v[44:47], v[116:119]\n"
"s_waitcnt lgkmcnt(2)\n"
"v_mfma_f32_16x16x32_bf16 v[48:51], v[160:163], v[32:35], v[48:51]\n"
"ds_read_b128 v[44:47], v0 offset:7232\n"
"v_mfma_f32_16x16x32_bf16 v[52:55], v[164:167], v[32:35], v[52:55]\n"
"v_add_u32 v0, s70, v1\n"
"v_mfma_f32_16x16x32_bf16 v[120:123], v[168:171], v[32:35], v[120:123]\n"
"v_rcp_f32 v24, v24\n"
"v_mfma_f32_16x16x32_bf16 v[124:127], v[172:175], v[32:35], v[124:127]\n"
"v_rcp_f32 v25, v25\n"
"v_mfma_f32_16x16x32_bf16 v[112:115], v[176:179], v[32:35], v[112:115]\n"
"v_rcp_f32 v26, v26\n"
"v_mfma_f32_16x16x32_bf16 v[116:119], v[180:183], v[32:35], v[116:119]\n"
"s_waitcnt lgkmcnt(2)\n"
"v_mfma_f32_16x16x32_bf16 v[48:51], v[184:187], v[36:39], v[48:51]\n"
"ds_read_b128 v[32:35], v0 offset:0\n"
"v_mfma_f32_16x16x32_bf16 v[52:55], v[188:191], v[36:39], v[52:55]\n"
"v_rcp_f32 v27, v27\n"
"v_mfma_f32_16x16x32_bf16 v[120:123], v[192:195], v[36:39], v[120:123]\n"
"v_rcp_f32 v28, v28\n"
"v_mfma_f32_16x16x32_bf16 v[124:127], v[196:199], v[36:39], v[124:127]\n"
"v_rcp_f32 v29, v29\n"
"v_mfma_f32_16x16x32_bf16 v[112:115], v[200:203], v[36:39], v[112:115]\n"
"v_rcp_f32 v30, v30\n"
"v_mfma_f32_16x16x32_bf16 v[116:119], v[204:207], v[36:39], v[116:119]\n"
"s_waitcnt lgkmcnt(2)\n"
"v_mfma_f32_16x16x32_bf16 v[48:51], v[208:211], v[40:43], v[48:51]\n"
"ds_read_b128 v[36:39], v0 offset:64\n"
"v_mfma_f32_16x16x32_bf16 v[52:55], v[212:215], v[40:43], v[52:55]\n"
"v_rcp_f32 v31, v31\n"
"v_mfma_f32_16x16x32_bf16 v[120:123], v[216:219], v[40:43], v[120:123]\n"
"v_mul_f32 v72, v72, v24\n"
"v_mul_f32 v73, v73, v25\n"
"v_mfma_f32_16x16x32_bf16 v[124:127], v[220:223], v[40:43], v[124:127]\n"
"v_mul_f32 v74, v74, v26\n"
"v_mul_f32 v75, v75, v27\n"
"v_mfma_f32_16x16x32_bf16 v[112:115], v[224:227], v[40:43], v[112:115]\n"
"v_mul_f32 v76, v76, v28\n"
"v_mul_f32 v77, v77, v29\n"
"v_mfma_f32_16x16x32_bf16 v[116:119], v[228:231], v[40:43], v[116:119]\n"
"v_mul_f32 v78, v78, v30\n"
"s_waitcnt lgkmcnt(2)\n"
"v_mfma_f32_16x16x32_bf16 v[48:51], v[232:235], v[44:47], v[48:51]\n"
"ds_read_b128 v[40:43], v0 offset:1024\n"
"v_mul_f32 v79, v79, v31\n"
"v_mfma_f32_16x16x32_bf16 v[52:55], v[236:239], v[44:47], v[52:55]\n"
"v_cvt_pk_bf16_f32 v72, v72, v73\n"
"v_cvt_pk_bf16_f32 v73, v74, v75\n"
"v_mfma_f32_16x16x32_bf16 v[120:123], v[240:243], v[44:47], v[120:123]\n"
"v_cvt_pk_bf16_f32 v74, v76, v77\n"
"v_cvt_pk_bf16_f32 v75, v78, v79\n"
"v_mfma_f32_16x16x32_bf16 v[124:127], v[244:247], v[44:47], v[124:127]\n"
"buffer_store_dwordx4 v[72:75], v3, s[56:59], s82 offen offset:3072 sc1 nt\n"
"v_mfma_f32_16x16x32_bf16 v[112:115], v[248:251], v[44:47], v[112:115]\n"
"s_add_u32 s82, s82, 4096\n"
"v_mfma_f32_16x16x32_bf16 v[116:119], v[252:255], v[44:47], v[116:119]\n"
"v_fma_f32 v80, v80, v8, v16\n"
"s_waitcnt lgkmcnt(2)\n"
"v_mfma_f32_16x16x32_bf16 v[56:59], a[0:3], v[32:35], 0\n"
"ds_read_b128 v[44:47], v0 offset:1088\n"
"v_fma_f32 v81, v81, v9, v17\n"
"v_mfma_f32_16x16x32_bf16 v[60:63], a[4:7], v[32:35], 0\n"
"s_add_u32 s70, s70, 16448\n"
"v_fma_f32 v82, v82, v10, v18\n"
"v_mfma_f32_16x16x32_bf16 v[48:51], a[8:11], v[32:35], v[48:51]\n"
"v_fma_f32 v83, v83, v11, v19\n"
"v_fma_f32 v84, v84, v12, v20\n"
"v_mfma_f32_16x16x32_bf16 v[52:55], a[12:15], v[32:35], v[52:55]\n"
"s_cmp_eq_u32 s70, 148032\ns_cselect_b32 s70, 0, s70\n"
"v_mfma_f32_16x16x32_bf16 v[120:123], a[16:19], v[32:35], v[120:123]\n"
"v_fma_f32 v85, v85, v13, v21\n"
"v_mfma_f32_16x16x32_bf16 v[124:127], a[20:23], v[32:35], v[124:127]\n"
"v_fma_f32 v86, v86, v14, v22\n"
"s_waitcnt lgkmcnt(2)\n"
"v_mfma_f32_16x16x32_bf16 v[56:59], a[24:27], v[36:39], v[56:59]\n"
"ds_read_b128 v[32:35], v0 offset:2048\n"
"v_fma_f32 v87, v87, v15, v23\n"
"v_mfma_f32_16x16x32_bf16 v[60:63], a[28:31], v[36:39], v[60:63]\n"
"v_exp_f32 v24, v80\n"
"v_mfma_f32_16x16x32_bf16 v[48:51], a[32:35], v[36:39], v[48:51]\n"
"s_mov_b32 m0, s68\n"
"v_mfma_f32_16x16x32_bf16 v[52:55], a[36:39], v[36:39], v[52:55]\n"
"v_exp_f32 v25, v81\n"
"v_mfma_f32_16x16x32_bf16 v[120:123], a[40:43], v[36:39], v[120:123]\n"
"v_exp_f32 v26, v82\n"
"v_mfma_f32_16x16x32_bf16 v[124:127], a[44:47], v[36:39], v[124:127]\n"
"s_waitcnt lgkmcnt(2)\n"
"v_mfma_f32_16x16x32_bf16 v[56:59], a[48:51], v[40:43], v[56:59]\n"
"ds_read_b128 v[36:39], v0 offset:2112\n"
"v_mfma_f32_16x16x32_bf16 v[60:63], a[52:55], v[40:43], v[60:63]\n"
"buffer_load_dwordx4 v2, s[36:39], s79 offen nt lds\n"
"v_mfma_f32_16x16x32_bf16 v[48:51], a[56:59], v[40:43], v[48:51]\n"
"v_exp_f32 v27, v83\n"
"v_mfma_f32_16x16x32_bf16 v[52:55], a[60:63], v[40:43], v[52:55]\n"
"v_exp_f32 v28, v84\n"
"v_mfma_f32_16x16x32_bf16 v[120:123], a[64:67], v[40:43], v[120:123]\n"
"s_add_u32 m0, s68, 8256\n"
"v_mfma_f32_16x16x32_bf16 v[124:127], a[68:71], v[40:43], v[124:127]\n"
"s_waitcnt lgkmcnt(2)\n"
"v_mfma_f32_16x16x32_bf16 v[56:59], a[72:75], v[44:47], v[56:59]\n"
"ds_read_b128 v[40:43], v0 offset:3072\n"
"v_mfma_f32_16x16x32_bf16 v[60:63], a[76:79], v[44:47], v[60:63]\n"
"buffer_load_dwordx4 v2, s[40:43], s79 offen nt lds\n"
"v_mfma_f32_16x16x32_bf16 v[48:51], a[80:83], v[44:47], v[48:51]\n"
"v_exp_f32 v29, v85\n"
"v_mfma_f32_16x16x32_bf16 v[52:55], a[84:87], v[44:47], v[52:55]\n"
"v_exp_f32 v30, v86\n"
"v_mfma_f32_16x16x32_bf16 v[120:123], a[88:91], v[44:47], v[120:123]\n"
"v_exp_f32 v31, v87\n"
"v_mfma_f32_16x16x32_bf16 v[124:127], a[92:95], v[44:47], v[124:127]\n"
"v_fma_f32 v24, v24, s83, s83\n"
"s_waitcnt lgkmcnt(2)\n"
"v_mfma_f32_16x16x32_bf16 v[56:59], a[96:99], v[32:35], v[56:59]\n"
"ds_read_b128 v[44:47], v0 offset:3136\n"
"v_fma_f32 v25, v25, s83, s83\n"
"v_mfma_f32_16x16x32_bf16 v[60:63], a[100:103], v[32:35], v[60:63]\n"
"s_add_u32 s79, s79, 1024\n"
"v_fma_f32 v26, v26, s83, s83\n"
"v_mfma_f32_16x16x32_bf16 v[48:51], a[104:107], v[32:35], v[48:51]\n"
"v_fma_f32 v27, v27, s83, s83\n"
"v_fma_f32 v28, v28, s83, s83\n"
"v_mfma_f32_16x16x32_bf16 v[52:55], a[108:111], v[32:35], v[52:55]\n"
"s_add_u32 m0, s68, 1024\n"
"v_fma_f32 v29, v29, s83, s83\n"
"v_mfma_f32_16x16x32_bf16 v[120:123], a[112:115], v[32:35], v[120:123]\n"
"v_fma_f32 v30, v30, s83, s83\n"
"v_fma_f32 v31, v31, s83, s83\n"
"v_mfma_f32_16x16x32_bf16 v[124:127], a[116:119], v[32:35], v[124:127]\n"
"s_waitcnt lgkmcnt(2)\n"
"v_mfma_f32_16x16x32_bf16 v[56:59], a[120:123], v[36:39], v[56:59]\n"
"ds_read_b128 v[32:35], v0 offset:4096\n"
"v_mfma_f32_16x16x32_bf16 v[60:63], a[124:127], v[36:39], v[60:63]\n"
"buffer_load_dwordx4 v2, s[36:39], s80 offen nt lds\n"
"v_mfma_f32_16x16x32_bf16 v[48:51], a[128:131], v[36:39], v[48:51]\n"
"v_rcp_f32 v24, v24\n"
"v_mfma_f32_16x16x32_bf16 v[52:55], a[132:135], v[36:39], v[52:55]\n"
"v_rcp_f32 v25, v25\n"
"v_mfma_f32_16x16x32_bf16 v[120:123], a[136:139], v[36:39], v[120:123]\n"
"v_rcp_f32 v26, v26\n"
"v_mfma_f32_16x16x32_bf16 v[124:127], a[140:143], v[36:39], v[124:127]\n"
"s_waitcnt lgkmcnt(2)\n"
"v_mfma_f32_16x16x32_bf16 v[56:59], a[144:147], v[40:43], v[56:59]\n"
"ds_read_b128 v[36:39], v0 offset:4160\n"
"s_add_u32 m0, s68, 9280\n"
"v_mfma_f32_16x16x32_bf16 v[60:63], a[148:151], v[40:43], v[60:63]\n"
"v_rcp_f32 v27, v27\n"
"v_mfma_f32_16x16x32_bf16 v[48:51], a[152:155], v[40:43], v[48:51]\n"
"buffer_load_dwordx4 v2, s[40:43], s80 offen nt lds\n"
"v_mfma_f32_16x16x32_bf16 v[52:55], a[156:159], v[40:43], v[52:55]\n"
"v_rcp_f32 v28, v28\n"
"v_mfma_f32_16x16x32_bf16 v[120:123], a[160:163], v[40:43], v[120:123]\n"
"v_rcp_f32 v29, v29\n"
"v_mfma_f32_16x16x32_bf16 v[124:127], a[164:167], v[40:43], v[124:127]\n"
"s_waitcnt lgkmcnt(2)\n"
"v_mfma_f32_16x16x32_bf16 v[56:59], a[168:171], v[44:47], v[56:59]\n"
"ds_read_b128 v[40:43], v0 offset:5120\n"
"v_mfma_f32_16x16x32_bf16 v[60:63], a[172:175], v[44:47], v[60:63]\n"
"v_rcp_f32 v30, v30\n"
"v_mfma_f32_16x16x32_bf16 v[48:51], a[176:179], v[44:47], v[48:51]\n"
"s_add_u32 s80, s80, 1024\n"
"v_mfma_f32_16x16x32_bf16 v[52:55], a[180:183], v[44:47], v[52:55]\n"
"v_rcp_f32 v31, v31\n"
"v_mfma_f32_16x16x32_bf16 v[120:123], a[184:187], v[44:47], v[120:123]\n"
"s_add_u32 s68, s68, 16448\n"
"v_mul_f32 v80, v80, v24\n"
"v_mfma_f32_16x16x32_bf16 v[124:127], a[188:191], v[44:47], v[124:127]\n"
"v_mul_f32 v81, v81, v25\n"
"s_waitcnt lgkmcnt(2)\n"
"v_mfma_f32_16x16x32_bf16 v[56:59], a[192:195], v[32:35], v[56:59]\n"
"ds_read_b128 v[44:47], v0 offset:5184\n"
"v_mul_f32 v82, v82, v26\n"
"v_mfma_f32_16x16x32_bf16 v[60:63], a[196:199], v[32:35], v[60:63]\n"
"s_cmp_eq_u32 s68, s69\ns_cselect_b32 s68, s81, s68\n"
"v_mfma_f32_16x16x32_bf16 v[48:51], a[200:203], v[32:35], v[48:51]\n"
"v_mul_f32 v83, v83, v27\n"
"v_mul_f32 v84, v84, v28\n"
"v_mfma_f32_16x16x32_bf16 v[52:55], a[204:207], v[32:35], v[52:55]\n"
"v_mul_f32 v85, v85, v29\n"
"v_mul_f32 v86, v86, v30\n"
"v_mfma_f32_16x16x32_bf16 v[120:123], a[208:211], v[32:35], v[120:123]\n"
"v_mul_f32 v87, v87, v31\n"
"v_cvt_pk_bf16_f32 v80, v80, v81\n"
"v_mfma_f32_16x16x32_bf16 v[124:127], a[212:215], v[32:35], v[124:127]\n"
"v_cvt_pk_bf16_f32 v81, v82, v83\n"
"s_waitcnt lgkmcnt(2)\n"
"v_mfma_f32_16x16x32_bf16 v[56:59], a[216:219], v[36:39], v[56:59]\n"
"ds_read_b128 v[32:35], v0 offset:6144\n"
"v_cvt_pk_bf16_f32 v82, v84, v85\n"
"v_mfma_f32_16x16x32_bf16 v[60:63], a[220:223], v[36:39], v[60:63]\n"
"v_cvt_pk_bf16_f32 v83, v86, v87\n"
"v_mfma_f32_16x16x32_bf16 v[48:51], a[224:227], v[36:39], v[48:51]\n"
"buffer_store_dwordx4 v[80:83], v3, s[56:59], s82 offen offset:0 sc1 nt\n"
"v_mfma_f32_16x16x32_bf16 v[52:55], a[228:231], v[36:39], v[52:55]\n"
"v_fma_f32 v88, v88, v8, v16\n"
"v_fma_f32 v89, v89, v9, v17\n"
"v_mfma_f32_16x16x32_bf16 v[120:123], a[232:235], v[36:39], v[120:123]\n"
"v_fma_f32 v90, v90, v10, v18\n"
"v_fma_f32 v91, v91, v11, v19\n"
"v_mfma_f32_16x16x32_bf16 v[124:127], a[236:239], v[36:39], v[124:127]\n"
"s_waitcnt lgkmcnt(2)\n"
"v_mfma_f32_16x16x32_bf16 v[56:59], a[240:243], v[40:43], v[56:59]\n"
"ds_read_b128 v[36:39], v0 offset:6208\n"
"v_fma_f32 v92, v92, v12, v20\n"
"v_mfma_f32_16x16x32_bf16 v[60:63], a[244:247], v[40:43], v[60:63]\n"
"v_fma_f32 v93, v93, v13, v21\n"
"v_fma_f32 v94, v94, v14, v22\n"
"v_mfma_f32_16x16x32_bf16 v[48:51], a[248:251], v[40:43], v[48:51]\n"
"v_fma_f32 v95, v95, v15, v23\n"
"v_mfma_f32_16x16x32_bf16 v[52:55], a[252:255], v[40:43], v[52:55]\n"
"v_exp_f32 v24, v88\n"
"v_mfma_f32_16x16x32_bf16 v[120:123], v[128:131], v[40:43], v[120:123]\n"
"v_exp_f32 v25, v89\n"
"v_mfma_f32_16x16x32_bf16 v[124:127], v[132:135], v[40:43], v[124:127]\n"
"s_waitcnt lgkmcnt(2)\n"
"v_mfma_f32_16x16x32_bf16 v[56:59], v[136:139], v[44:47], v[56:59]\n"
"ds_read_b128 v[40:43], v0 offset:7168\n"
"v_mfma_f32_16x16x32_bf16 v[60:63], v[140:143], v[44:47], v[60:63]\n"
"v_exp_f32 v26, v90\n"
"v_mfma_f32_16x16x32_bf16 v[48:51], v[144:147], v[44:47], v[48:51]\n"
"v_exp_f32 v27, v91\n"
"v_mfma_f32_16x16x32_bf16 v[52:55], v[148:151], v[44:47], v[52:55]\n"
"v_exp_f32 v28, v92\n"
"v_mfma_f32_16x16x32_bf16 v[120:123], v[152:155], v[44:47], v[120:123]\n"
"v_exp_f32 v29, v93\n"
"v_mfma_f32_16x16x32_bf16 v[124:127], v[156:159], v[44:47], v[124:127]\n"
"s_waitcnt vmcnt(12)\n"
"s_barrier\n"
"s_waitcnt lgkmcnt(2)\n"
"v_mfma_f32_16x16x32_bf16 v[56:59], v[160:163], v[32:35], v[56:59]\n"
"ds_read_b128 v[44:47], v0 offset:7232\n"
"v_mfma_f32_16x16x32_bf16 v[60:63], v[164:167], v[32:35], v[60:63]\n"
"v_add_u32 v0, s70, v1\n"
"v_mfma_f32_16x16x32_bf16 v[48:51], v[168:171], v[32:35], v[48:51]\n"
"v_exp_f32 v30, v94\n"
"v_mfma_f32_16x16x32_bf16 v[52:55], v[172:175], v[32:35], v[52:55]\n"
"v_exp_f32 v31, v95\n"
"v_mfma_f32_16x16x32_bf16 v[120:123], v[176:179], v[32:35], v[120:123]\n"
"v_fma_f32 v24, v24, s83, s83\n"
"v_fma_f32 v25, v25, s83, s83\n"
"v_mfma_f32_16x16x32_bf16 v[124:127], v[180:183], v[32:35], v[124:127]\n"
"v_fma_f32 v26, v26, s83, s83\n"
"s_waitcnt lgkmcnt(2)\n"
"v_mfma_f32_16x16x32_bf16 v[56:59], v[184:187], v[36:39], v[56:59]\n"
"ds_read_b128 v[32:35], v0 offset:0\n"
"v_fma_f32 v27, v27, s83, s83\n"
"v_mfma_f32_16x16x32_bf16 v[60:63], v[188:191], v[36:39], v[60:63]\n"
"v_fma_f32 v28, v28, s83, s83\n"
"v_mfma_f32_16x16x32_bf16 v[48:51], v[192:195], v[36:39], v[48:51]\n"
"v_fma_f32 v29, v29, s83, s83\n"
"v_fma_f32 v30, v30, s83, s83\n"
"v_mfma_f32_16x16x32_bf16 v[52:55], v[196:199], v[36:39], v[52:55]\n"
"v_fma_f32 v31, v31, s83, s83\n"
"v_mfma_f32_16x16x32_bf16 v[120:123], v[200:203], v[36:39], v[120:123]\n"
"v_rcp_f32 v24, v24\n"
"v_mfma_f32_16x16x32_bf16 v[124:127], v[204:207], v[36:39], v[124:127]\n"
"s_waitcnt lgkmcnt(2)\n"
"v_mfma_f32_16x16x32_bf16 v[56:59], v[208:211], v[40:43], v[56:59]\n"
"ds_read_b128 v[36:39], v0 offset:64\n"
"v_mfma_f32_16x16x32_bf16 v[60:63], v[212:215], v[40:43], v[60:63]\n"
"v_rcp_f32 v25, v25\n"
"v_mfma_f32_16x16x32_bf16 v[48:51], v[216:219], v[40:43], v[48:51]\n"
"v_rcp_f32 v26, v26\n"
"v_mfma_f32_16x16x32_bf16 v[52:55], v[220:223], v[40:43], v[52:55]\n"
"v_rcp_f32 v27, v27\n"
"v_mfma_f32_16x16x32_bf16 v[120:123], v[224:227], v[40:43], v[120:123]\n"
"v_rcp_f32 v28, v28\n"
"v_mfma_f32_16x16x32_bf16 v[124:127], v[228:231], v[40:43], v[124:127]\n"
"s_waitcnt lgkmcnt(2)\n"
"v_mfma_f32_16x16x32_bf16 v[56:59], v[232:235], v[44:47], v[56:59]\n"
"ds_read_b128 v[40:43], v0 offset:1024\n"
"v_mfma_f32_16x16x32_bf16 v[60:63], v[236:239], v[44:47], v[60:63]\n"
"v_rcp_f32 v29, v29\n"
"v_mfma_f32_16x16x32_bf16 v[48:51], v[240:243], v[44:47], v[48:51]\n"
"v_rcp_f32 v30, v30\n"
"v_mfma_f32_16x16x32_bf16 v[52:55], v[244:247], v[44:47], v[52:55]\n"
"v_rcp_f32 v31, v31\n"
"v_mfma_f32_16x16x32_bf16 v[120:123], v[248:251], v[44:47], v[120:123]\n"
"v_mul_f32 v88, v88, v24\n"
"v_mul_f32 v89, v89, v25\n"
"v_mfma_f32_16x16x32_bf16 v[124:127], v[252:255], v[44:47], v[124:127]\n"
"v_mul_f32 v90, v90, v26\n"
"s_waitcnt lgkmcnt(2)\n"
"v_mfma_f32_16x16x32_bf16 v[64:67], a[0:3], v[32:35], 0\n"
"ds_read_b128 v[44:47], v0 offset:1088\n"
"v_mfma_f32_16x16x32_bf16 v[68:71], a[4:7], v[32:35], 0\n"
"s_add_u32 s70, s70, 16448\n"
"v_mul_f32 v91, v91, v27\n"
"v_mfma_f32_16x16x32_bf16 v[56:59], a[8:11], v[32:35], v[56:59]\n"
"v_mul_f32 v92, v92, v28\n"
"v_mul_f32 v93, v93, v29\n"
"v_mfma_f32_16x16x32_bf16 v[60:63], a[12:15], v[32:35], v[60:63]\n"
"s_cmp_eq_u32 s70, 148032\ns_cselect_b32 s70, 0, s70\n"
"v_mfma_f32_16x16x32_bf16 v[48:51], a[16:19], v[32:35], v[48:51]\n"
"v_mul_f32 v94, v94, v30\n"
"v_mfma_f32_16x16x32_bf16 v[52:55], a[20:23], v[32:35], v[52:55]\n"
"v_mul_f32 v95, v95, v31\n"
"s_waitcnt lgkmcnt(2)\n"
"v_mfma_f32_16x16x32_bf16 v[64:67], a[24:27], v[36:39], v[64:67]\n"
"ds_read_b128 v[32:35], v0 offset:2048\n"
"v_cvt_pk_bf16_f32 v88, v88, v89\n"
"v_mfma_f32_16x16x32_bf16 v[68:71], a[28:31], v[36:39], v[68:71]\n"
"v_cvt_pk_bf16_f32 v89, v90, v91\n"
"v_cvt_pk_bf16_f32 v90, v92, v93\n"
"v_mfma_f32_16x16x32_bf16 v[56:59], a[32:35], v[36:39], v[56:59]\n"
"s_mov_b32 m0, s68\n"
"v_mfma_f32_16x16x32_bf16 v[60:63], a[36:39], v[36:39], v[60:63]\n"
"v_cvt_pk_bf16_f32 v91, v94, v95\n"
"v_mfma_f32_16x16x32_bf16 v[48:51], a[40:43], v[36:39], v[48:51]\n"
"buffer_store_dwordx4 v[88:91], v3, s[56:59], s82 offen offset:1024 sc1 nt\n"
"v_mfma_f32_16x16x32_bf16 v[52:55], a[44:47], v[36:39], v[52:55]\n"
"v_fma_f32 v96, v96, v8, v16\n"
"s_waitcnt lgkmcnt(2)\n"
"v_mfma_f32_16x16x32_bf16 v[64:67], a[48:51], v[40:43], v[64:67]\n"
"ds_read_b128 v[36:39], v0 offset:2112\n"
"v_fma_f32 v97, v97, v9, v17\n"
"v_mfma_f32_16x16x32_bf16 v[68:71], a[52:55], v[40:43], v[68:71]\n"
"buffer_load_dwordx4 v2, s[36:39], s79 offen nt lds\n"
"v_mfma_f32_16x16x32_bf16 v[56:59], a[56:59], v[40:43], v[56:59]\n"
"v_fma_f32 v98, v98, v10, v18\n"
"v_fma_f32 v99, v99, v11, v19\n"
"v_mfma_f32_16x16x32_bf16 v[60:63], a[60:63], v[40:43], v[60:63]\n"
"v_fma_f32 v100, v100, v12, v20\n"
"v_mfma_f32_16x16x32_bf16 v[48:51], a[64:67], v[40:43], v[48:51]\n"
"s_add_u32 m0, s68, 8256\n"
"v_fma_f32 v101, v101, v13, v21\n"
"v_mfma_f32_16x16x32_bf16 v[52:55], a[68:71], v[40:43], v[52:55]\n"
"v_fma_f32 v102, v102, v14, v22\n"
"s_waitcnt lgkmcnt(2)\n"
"v_mfma_f32_16x16x32_bf16 v[64:67], a[72:75], v[44:47], v[64:67]\n"
"ds_read_b128 v[40:43], v0 offset:3072\n"
"v_fma_f32 v103, v103, v15, v23\n"
"v_mfma_f32_16x16x32_bf16 v[68:71], a[76:79], v[44:47], v[68:71]\n"
"buffer_load_dwordx4 v2, s[40:43], s79 offen nt lds\n"
"v_mfma_f32_16x16x32_bf16 v[56:59], a[80:83], v[44:47], v[56:59]\n"
"v_exp_f32 v24, v96\n"
"v_mfma_f32_16x16x32_bf16 v[60:63], a[84:87], v[44:47], v[60:63]\n"
"v_exp_f32 v25, v97\n"
"v_mfma_f32_16x16x32_bf16 v[48:51], a[88:91], v[44:47], v[48:51]\n"
"v_exp_f32 v26, v98\n"
"v_mfma_f32_16x16x32_bf16 v[52:55], a[92:95], v[44:47], v[52:55]\n"
"s_waitcnt lgkmcnt(2)\n"
"v_mfma_f32_16x16x32_bf16 v[64:67], a[96:99], v[32:35], v[64:67]\n"
"ds_read_b128 v[44:47], v0 offset:3136\n"
"v_mfma_f32_16x16x32_bf16 v[68:71], a[100:103], v[32:35], v[68:71]\n"
"s_add_u32 s79, s79, 1024\n"
"v_mfma_f32_16x16x32_bf16 v[56:59], a[104:107], v[32:35], v[56:59]\n"
"v_exp_f32 v27, v99\n"
"v_mfma_f32_16x16x32_bf16 v[60:63], a[108:111], v[32:35], v[60:63]\n"
"s_add_u32 m0, s68, 1024\n"
"v_mfma_f32_16x16x32_bf16 v[48:51], a[112:115], v[32:35], v[48:51]\n"
"v_exp_f32 v28, v100\n"
"v_mfma_f32_16x16x32_bf16 v[52:55], a[116:119], v[32:35], v[52:55]\n"
"s_waitcnt lgkmcnt(2)\n"
"v_mfma_f32_16x16x32_bf16 v[64:67], a[120:123], v[36:39], v[64:67]\n"
"ds_read_b128 v[32:35], v0 offset:4096\n"
"v_mfma_f32_16x16x32_bf16 v[68:71], a[124:127], v[36:39], v[68:71]\n"
"buffer_load_dwordx4 v2, s[36:39], s80 offen nt lds\n"
"v_mfma_f32_16x16x32_bf16 v[56:59], a[128:131], v[36:39], v[56:59]\n"
"v_exp_f32 v29, v101\n"
"v_mfma_f32_16x16x32_bf16 v[60:63], a[132:135], v[36:39], v[60:63]\n"
"v_exp_f32 v30, v102\n"
"v_mfma_f32_16x16x32_bf16 v[48:51], a[136:139], v[36:39], v[48:51]\n"
"v_exp_f32 v31, v103\n"
"v_mfma_f32_16x16x32_bf16 v[52:55], a[140:143], v[36:39], v[52:55]\n"
"v_fma_f32 v24, v24, s83, s83\n"
"s_waitcnt lgkmcnt(2)\n"
"v_mfma_f32_16x16x32_bf16 v[64:67], a[144:147], v[40:43], v[64:67]\n"
"ds_read_b128 v[36:39], v0 offset:4160\n"
"s_add_u32 m0, s68, 9280\n"
"v_mfma_f32_16x16x32_bf16 v[68:71], a[148:151], v[40:43], v[68:71]\n"
"v_fma_f32 v25, v25, s83, s83\n"
"v_fma_f32 v26, v26, s83, s83\n"
"v_mfma_f32_16x16x32_bf16 v[56:59], a[152:155], v[40:43], v[56:59]\n"
"buffer_load_dwordx4 v2, s[40:43], s80 offen nt lds\n"
"v_mfma_f32_16x16x32_bf16 v[60:63], a[156:159], v[40:43], v[60:63]\n"
"v_fma_f32 v27, v27, s83, s83\n"
"v_fma_f32 v28, v28, s83, s83\n"
"v_mfma_f32_16x16x32_bf16 v[48:51], a[160:163], v[40:43], v[48:51]\n"
"v_fma_f32 v29, v29, s83, s83\n"
"v_fma_f32 v30, v30, s83, s83\n"
"v_mfma_f32_16x16x32_bf16 v[52:55], a[164:167], v[40:43], v[52:55]\n"
"v_fma_f32 v31, v31, s83, s83\n"
"s_waitcnt lgkmcnt(2)\n"
"v_mfma_f32_16x16x32_bf16 v[64:67], a[168:171], v[44:47], v[64:67]\n"
"ds_read_b128 v[40:43], v0 offset:5120\n"
"v_mfma_f32_16x16x32_bf16 v[68:71], a[172:175], v[44:47], v[68:71]\n"
"v_rcp_f32 v24, v24\n"
"v_mfma_f32_16x16x32_bf16 v[56:59], a[176:179], v[44:47], v[56:59]\n"
"s_add_u32 s80, s80, 1024\n"
"v_mfma_f32_16x16x32_bf16 v[60:63], a[180:183], v[44:47], v[60:63]\n"
"v_rcp_f32 v25, v25\n"
"v_mfma_f32_16x16x32_bf16 v[48:51], a[184:187], v[44:47], v[48:51]\n"
"s_add_u32 s68, s68, 16448\n"
"v_mfma_f32_16x16x32_bf16 v[52:55], a[188:191], v[44:47], v[52:55]\n"
"s_waitcnt lgkmcnt(2)\n"
"v_mfma_f32_16x16x32_bf16 v[64:67], a[192:195], v[32:35], v[64:67]\n"
"ds_read_b128 v[44:47], v0 offset:5184\n"
"v_mfma_f32_16x16x32_bf16 v[68:71], a[196:199], v[32:35], v[68:71]\n"
"s_cmp_eq_u32 s68, s69\ns_cselect_b32 s68, s81, s68\n"
"v_mfma_f32_16x16x32_bf16 v[56:59], a[200:203], v[32:35], v[56:59]\n"
"v_rcp_f32 v26, v26\n"
"v_mfma_f32_16x16x32_bf16 v[60:63], a[204:207], v[32:35], v[60:63]\n"
"v_rcp_f32 v27, v27\n"
"v_mfma_f32_16x16x32_bf16 v[48:51], a[208:211], v[32:35], v[48:51]\n"
"v_rcp_f32 v28, v28\n"
"v_mfma_f32_16x16x32_bf16 v[52:55], a[212:215], v[32:35], v[52:55]\n"
"s_waitcnt lgkmcnt(2)\n"
"v_mfma_f32_16x16x32_bf16 v[64:67], a[216:219], v[36:39], v[64:67]\n"
"ds_read_b128 v[32:35], v0 offset:6144\n"
"v_mfma_f32_16x16x32_bf16 v[68:71], a[220:223], v[36:39], v[68:71]\n"
"v_rcp_f32 v29, v29\n"
"v_mfma_f32_16x16x32_bf16 v[56:59], a[224:227], v[36:39], v[56:59]\n"
"v_rcp_f32 v30, v30\n"
"v_mfma_f32_16x16x32_bf16 v[60:63], a[228:231], v[36:39], v[60:63]\n"
"v_rcp_f32 v31, v31\n"
"v_mfma_f32_16x16x32_bf16 v[48:51], a[232:235], v[36:39], v[48:51]\n"
"v_mul_f32 v96, v96, v24\n"
"v_mul_f32 v97, v97, v25\n"
"v_mfma_f32_16x16x32_bf16 v[52:55], a[236:239], v[36:39], v[52:55]\n"
"v_mul_f32 v98, v98, v26\n"
"s_waitcnt lgkmcnt(2)\n"
"v_mfma_f32_16x16x32_bf16 v[64:67], a[240:243], v[40:43], v[64:67]\n"
"ds_read_b128 v[36:39], v0 offset:6208\n"
"v_mul_f32 v99, v99, v27\n"
"v_mfma_f32_16x16x32_bf16 v[68:71], a[244:247], v[40:43], v[68:71]\n"
"v_mul_f32 v100, v100, v28\n"
"v_mul_f32 v101, v101, v29\n"
"v_mfma_f32_16x16x32_bf16 v[56:59], a[248:251], v[40:43], v[56:59]\n"
"v_mul_f32 v102, v102, v30\n"
"v_mul_f32 v103, v103, v31\n"
"v_mfma_f32_16x16x32_bf16 v[60:63], a[252:255], v[40:43], v[60:63]\n"
"v_cvt_pk_bf16_f32 v96, v96, v97\n"
"v_cvt_pk_bf16_f32 v97, v98, v99\n"
"v_mfma_f32_16x16x32_bf16 v[48:51], v[128:131], v[40:43], v[48:51]\n"
"v_cvt_pk_bf16_f32 v98, v100, v101\n"
"v_cvt_pk_bf16_f32 v99, v102, v103\n"
"v_mfma_f32_16x16x32_bf16 v[52:55], v[132:135], v[40:43], v[52:55]\n"
"buffer_store_dwordx4 v[96:99], v3, s[56:59], s82 offen offset:2048 sc1 nt\n"
"s_waitcnt lgkmcnt(2)\n"
"v_mfma_f32_16x16x32_bf16 v[64:67], v[136:139], v[44:47], v[64:67]\n"
"ds_read_b128 v[40:43], v0 offset:7168\n"
"v_mfma_f32_16x16x32_bf16 v[68:71], v[140:143], v[44:47], v[68:71]\n"
"v_fma_f32 v104, v104, v8, v16\n"
"v_fma_f32 v105, v105, v9, v17\n"
"v_mfma_f32_16x16x32_bf16 v[56:59], v[144:147], v[44:47], v[56:59]\n"
"v_fma_f32 v106, v106, v10, v18\n"
"v_fma_f32 v107, v107, v11, v19\n"
"v_mfma_f32_16x16x32_bf16 v[60:63], v[148:151], v[44:47], v[60:63]\n"
"v_fma_f32 v108, v108, v12, v20\n"
"v_mfma_f32_16x16x32_bf16 v[48:51], v[152:155], v[44:47], v[48:51]\n"
"v_fma_f32 v109, v109, v13, v21\n"
"v_fma_f32 v110, v110, v14, v22\n"
"v_mfma_f32_16x16x32_bf16 v[52:55], v[156:159], v[44:47], v[52:55]\n"
"v_fma_f32 v111, v111, v15, v23\n"
"s_waitcnt lgkmcnt(2)\n"
"v_mfma_f32_16x16x32_bf16 v[64:67], v[160:163], v[32:35], v[64:67]\n"
"ds_read_b128 v[44:47], v0 offset:7232\n"
"v_mfma_f32_16x16x32_bf16 v[68:71], v[164:167], v[32:35], v[68:71]\n"
"v_add_u32 v0, s70, v1\n"
"v_mfma_f32_16x16x32_bf16 v[56:59], v[168:171], v[32:35], v[56:59]\n"
"v_exp_f32 v24, v104\n"
"v_mfma_f32_16x16x32_bf16 v[60:63], v[172:175], v[32:35], v[60:63]\n"
"v_exp_f32 v25, v105\n"
"v_mfma_f32_16x16x32_bf16 v[48:51], v[176:179], v[32:35], v[48:51]\n"
"v_exp_f32 v26, v106\n"
"v_mfma_f32_16x16x32_bf16 v[52:55], v[180:183], v[32:35], v[52:55]\n"
"s_waitcnt lgkmcnt(2)\n"
"v_mfma_f32_16x16x32_bf16 v[64:67], v[184:187], v[36:39], v[64:67]\n"
"ds_read_b128 v[32:35], v0 offset:0\n"
"v_mfma_f32_16x16x32_bf16 v[68:71], v[188:191], v[36:39], v[68:71]\n"
"v_exp_f32 v27, v107\n"
"v_mfma_f32_16x16x32_bf16 v[56:59], v[192:195], v[36:39], v[56:59]\n"
"v_exp_f32 v28, v108\n"
"v_mfma_f32_16x16x32_bf16 v[60:63], v[196:199], v[36:39], v[60:63]\n"
"v_exp_f32 v29, v109\n"
"v_mfma_f32_16x16x32_bf16 v[48:51], v[200:203], v[36:39], v[48:51]\n"
"v_exp_f32 v30, v110\n"
"v_mfma_f32_16x16x32_bf16 v[52:55], v[204:207], v[36:39], v[52:55]\n"
"s_waitcnt lgkmcnt(2)\n"
"v_mfma_f32_16x16x32_bf16 v[64:67], v[208:211], v[40:43], v[64:67]\n"
"ds_read_b128 v[36:39], v0 offset:64\n"
"v_mfma_f32_16x16x32_bf16 v[68:71], v[212:215], v[40:43], v[68:71]\n"
"v_exp_f32 v31, v111\n"
"v_mfma_f32_16x16x32_bf16 v[56:59], v[216:219], v[40:43], v[56:59]\n"
"v_fma_f32 v24, v24, s83, s83\n"
"v_fma_f32 v25, v25, s83, s83\n"
"v_mfma_f32_16x16x32_bf16 v[60:63], v[220:223], v[40:43], v[60:63]\n"
"v_fma_f32 v26, v26, s83, s83\n"
"v_fma_f32 v27, v27, s83, s83\n"
"v_mfma_f32_16x16x32_bf16 v[48:51], v[224:227], v[40:43], v[48:51]\n"
"v_fma_f32 v28, v28, s83, s83\n"
"v_fma_f32 v29, v29, s83, s83\n"
"v_mfma_f32_16x16x32_bf16 v[52:55], v[228:231], v[40:43], v[52:55]\n"
"v_fma_f32 v30, v30, s83, s83\n"
"s_waitcnt lgkmcnt(2)\n"
"v_mfma_f32_16x16x32_bf16 v[64:67], v[232:235], v[44:47], v[64:67]\n"
"ds_read_b128 v[40:43], v0 offset:1024\n"
"v_fma_f32 v31, v31, s83, s83\n"
"v_mfma_f32_16x16x32_bf16 v[68:71], v[236:239], v[44:47], v[68:71]\n"
"v_rcp_f32 v24, v24\n"
"v_mfma_f32_16x16x32_bf16 v[56:59], v[240:243], v[44:47], v[56:59]\n"
"v_rcp_f32 v25, v25\n"
"v_mfma_f32_16x16x32_bf16 v[60:63], v[244:247], v[44:47], v[60:63]\n"
"v_rcp_f32 v26, v26\n"
"v_mfma_f32_16x16x32_bf16 v[48:51], v[248:251], v[44:47], v[48:51]\n"
"v_mfma_f32_16x16x32_bf16 v[52:55], v[252:255], v[44:47], v[52:55]\n"
"s_waitcnt lgkmcnt(2)\n"
"v_mfma_f32_16x16x32_bf16 v[72:75], a[0:3], v[32:35], 0\n"
"ds_read_b128 v[44:47], v0 offset:1088\n"
"v_mfma_f32_16x16x32_bf16 v[76:79], a[4:7], v[32:35], 0\n"
"s_add_u32 s70, s70, 16448\n"
"v_mfma_f32_16x16x32_bf16 v[64:67], a[8:11], v[32:35], v[64:67]\n"
"v_rcp_f32 v27, v27\n"
"v_mfma_f32_16x16x32_bf16 v[68:71], a[12:15], v[32:35], v[68:71]\n"
"s_cmp_eq_u32 s70, 148032\ns_cselect_b32 s70, 0, s70\n"
"v_mfma_f32_16x16x32_bf16 v[56:59], a[16:19], v[32:35], v[56:59]\n"
"v_rcp_f32 v28, v28\n"
"v_mfma_f32_16x16x32_bf16 v[60:63], a[20:23], v[32:35], v[60:63]\n"
"s_waitcnt lgkmcnt(2)\n"
"v_mfma_f32_16x16x32_bf16 v[72:75], a[24:27], v[36:39], v[72:75]\n"
"ds_read_b128 v[32:35], v0 offset:2048\n"
"v_mfma_f32_16x16x32_bf16 v[76:79], a[28:31], v[36:39], v[76:79]\n"
"v_rcp_f32 v29, v29\n"
"v_mfma_f32_16x16x32_bf16 v[64:67], a[32:35], v[36:39], v[64:67]\n"
"s_mov_b32 m0, s68\n"
"v_mfma_f32_16x16x32_bf16 v[68:71], a[36:39], v[36:39], v[68:71]\n"
"v_rcp_f32 v30, v30\n"
"v_mfma_f32_16x16x32_bf16 v[56:59], a[40:43], v[36:39], v[56:59]\n"
"v_rcp_f32 v31, v31\n"
"v_mfma_f32_16x16x32_bf16 v[60:63], a[44:47], v[36:39], v[60:63]\n"
"v_mul_f32 v104, v104, v24\n"
"s_waitcnt lgkmcnt(2)\n"
"v_mfma_f32_16x16x32_bf16 v[72:75], a[48:51], v[40:43], v[72:75]\n"
"ds_read_b128 v[36:39], v0 offset:2112\n"
"v_mul_f32 v105, v105, v25\n"
"v_mfma_f32_16x16x32_bf16 v[76:79], a[52:55], v[40:43], v[76:79]\n"
"buffer_load_dwordx4 v2, s[36:39], s79 offen nt lds\n"
"v_mfma_f32_16x16x32_bf16 v[64:67], a[56:59], v[40:43], v[64:67]\n"
"v_mul_f32 v106, v106, v26\n"
"v_mul_f32 v107, v107, v27\n"
"v_mfma_f32_16x16x32_bf16 v[68:71], a[60:63], v[40:43], v[68:71]\n"
"v_mul_f32 v108, v108, v28\n"
"v_mul_f32 v109, v109, v29\n"
"v_mfma_f32_16x16x32_bf16 v[56:59], a[64:67], v[40:43], v[56:59]\n"
"s_add_u32 m0, s68, 8256\n"
"v_mul_f32 v110, v110, v30\n"
"v_mfma_f32_16x16x32_bf16 v[60:63], a[68:71], v[40:43], v[60:63]\n"
"v_mul_f32 v111, v111, v31\n"
"s_waitcnt lgkmcnt(2)\n"
"v_mfma_f32_16x16x32_bf16 v[72:75], a[72:75], v[44:47], v[72:75]\n"
"ds_read_b128 v[40:43], v0 offset:3072\n"
"v_cvt_pk_bf16_f32 v104, v104, v105\n"
"v_mfma_f32_16x16x32_bf16 v[76:79], a[76:79], v[44:47], v[76:79]\n"
"buffer_load_dwordx4 v2, s[40:43], s79 offen nt lds\n"
"v_mfma_f32_16x16x32_bf16 v[64:67], a[80:83], v[44:47], v[64:67]\n"
"v_cvt_pk_bf16_f32 v105, v106, v107\n"
"v_cvt_pk_bf16_f32 v106, v108, v109\n"
"v_mfma_f32_16x16x32_bf16 v[68:71], a[84:87], v[44:47], v[68:71]\n"
"v_cvt_pk_bf16_f32 v107, v110, v111\n"
"v_mfma_f32_16x16x32_bf16 v[56:59], a[88:91], v[44:47], v[56:59]\n"
"buffer_store_dwordx4 v[104:107], v3, s[56:59], s82 offen offset:3072 sc1 nt\n"
"s_add_u32 s82, s82, 4096\n"
"v_mfma_f32_16x16x32_bf16 v[60:63], a[92:95], v[44:47], v[60:63]\n"
"v_fma_f32 v112, v112, v8, v16\n"
"s_waitcnt lgkmcnt(2)\n"
"v_mfma_f32_16x16x32_bf16 v[72:75], a[96:99], v[32:35], v[72:75]\n"
"ds_read_b128 v[44:47], v0 offset:3136\n"
"v_fma_f32 v113, v113, v9, v17\n"
"v_mfma_f32_16x16x32_bf16 v[76:79], a[100:103], v[32:35], v[76:79]\n"
"s_add_u32 s79, s79, 1024\n"
"v_fma_f32 v114, v114, v10, v18\n"
"v_mfma_f32_16x16x32_bf16 v[64:67], a[104:107], v[32:35], v[64:67]\n"
"v_fma_f32 v115, v115, v11, v19\n"
"v_fma_f32 v116, v116, v12, v20\n"
"v_mfma_f32_16x16x32_bf16 v[68:71], a[108:111], v[32:35], v[68:71]\n"
"s_add_u32 m0, s68, 1024\n"
"v_mfma_f32_16x16x32_bf16 v[56:59], a[112:115], v[32:35], v[56:59]\n"
"v_fma_f32 v117, v117, v13, v21\n"
"v_fma_f32 v118, v118, v14, v22\n"
"v_mfma_f32_16x16x32_bf16 v[60:63], a[116:119], v[32:35], v[60:63]\n"
"v_fma_f32 v119, v119, v15, v23\n"
"s_waitcnt lgkmcnt(2)\n"
"v_mfma_f32_16x16x32_bf16 v[72:75], a[120:123], v[36:39], v[72:75]\n"
"ds_read_b128 v[32:35], v0 offset:4096\n"
"v_mfma_f32_16x16x32_bf16 v[76:79], a[124:127], v[36:39], v[76:79]\n"
"buffer_load_dwordx4 v2, s[36:39], s80 offen nt lds\n"
"v_mfma_f32_16x16x32_bf16 v[64:67], a[128:131], v[36:39], v[64:67]\n"
"v_exp_f32 v24, v112\n"
"v_mfma_f32_16x16x32_bf16 v[68:71], a[132:135], v[36:39], v[68:71]\n"
"v_exp_f32 v25, v113\n"
"v_mfma_f32_16x16x32_bf16 v[56:59], a[136:139], v[36:39], v[56:59]\n"
"v_exp_f32 v26, v114\n"
"v_mfma_f32_16x16x32_bf16 v[60:63], a[140:143], v[36:39], v[60:63]\n"
"s_waitcnt lgkmcnt(2)\n"
"v_mfma_f32_16x16x32_bf16 v[72:75], a[144:147], v[40:43], v[72:75]\n"
"ds_read_b128 v[36:39], v0 offset:4160\n"
"s_add_u32 m0, s68, 9280\n"
"v_mfma_f32_16x16x32_bf16 v[76:79], a[148:151], v[40:43], v[76:79]\n"
"v_exp_f32 v27, v115\n"
"v_mfma_f32_16x16x32_bf16 v[64:67], a[152:155], v[40:43], v[64:67]\n"
"buffer_load_dwordx4 v2, s[40:43], s80 offen nt lds\n"
"v_mfma_f32_16x16x32_bf16 v[68:71], a[156:159], v[40:43], v[68:71]\n"
"v_exp_f32 v28, v116\n"
"v_mfma_f32_16x16x32_bf16 v[56:59], a[160:163], v[40:43], v[56:59]\n"
"v_exp_f32 v29, v117\n"
"v_mfma_f32_16x16x32_bf16 v[60:63], a[164:167], v[40:43], v[60:63]\n"
"s_waitcnt lgkmcnt(2)\n"
"v_mfma_f32_16x16x32_bf16 v[72:75], a[168:171], v[44:47], v[72:75]\n"
"ds_read_b128 v[40:43], v0 offset:5120\n"
"v_mfma_f32_16x16x32_bf16 v[76:79], a[172:175], v[44:47], v[76:79]\n"
"v_exp_f32 v30, v118\n"
"v_mfma_f32_16x16x32_bf16 v[64:67], a[176:179], v[44:47], v[64:67]\n"
"s_add_u32 s80, s80, 1024\n"
"v_mfma_f32_16x16x32_bf16 v[68:71], a[180:183], v[44:47], v[68:71]\n"
"v_exp_f32 v31, v119\n"
"v_mfma_f32_16x16x32_bf16 v[56:59], a[184:187], v[44:47], v[56:59]\n"
"s_add_u32 s68, s68, 16448\n"
"v_fma_f32 v24, v24, s83, s83\n"
"v_mfma_f32_16x16x32_bf16 v[60:63], a[188:191], v[44:47], v[60:63]\n"
"v_fma_f32 v25, v25, s83, s83\n"
"s_waitcnt lgkmcnt(2)\n"
"v_mfma_f32_16x16x32_bf16 v[72:75], a[192:195], v[32:35], v[72:75]\n"
"ds_read_b128 v[44:47], v0 offset:5184\n"
"v_fma_f32 v26, v26, s83, s83\n"
"v_mfma_f32_16x16x32_bf16 v[76:79], a[196:199], v[32:35], v[76:79]\n"
"s_cmp_eq_u32 s68, s69\ns_cselect_b32 s68, s81, s68\n"
"v_mfma_f32_16x16x32_bf16 v[64:67], a[200:203], v[32:35], v[64:67]\n"
"v_fma_f32 v27, v27, s83, s83\n"
"v_fma_f32 v28, v28, s83, s83\n"
"v_mfma_f32_16x16x32_bf16 v[68:71], a[204:207], v[32:35], v[68:71]\n"
"v_fma_f32 v29, v29, s83, s83\n"
"v_fma_f32 v30, v30, s83, s83\n"
"v_mfma_f32_16x16x32_bf16 v[56:59], a[208:211], v[32:35], v[56:59]\n"
"v_fma_f32 v31, v31, s83, s83\n"
"v_mfma_f32_16x16x32_bf16 v[60:63], a[212:215], v[32:35], v[60:63]\n"
"s_waitcnt lgkmcnt(2)\n"
"v_mfma_f32_16x16x32_bf16 v[72:75], a[216:219], v[36:39], v[72:75]\n"
"ds_read_b128 v[32:35], v0 offset:6144\n"
"v_mfma_f32_16x16x32_bf16 v[76:79], a[220:223], v[36:39], v[76:79]\n"
"v_rcp_f32 v24, v24\n"
"v_mfma_f32_16x16x32_bf16 v[64:67], a[224:227], v[36:39], v[64:67]\n"
"v_rcp_f32 v25, v25\n"
"v_mfma_f32_16x16x32_bf16 v[68:71], a[228:231], v[36:39], v[68:71]\n"
"v_rcp_f32 v26, v26\n"
"v_mfma_f32_16x16x32_bf16 v[56:59], a[232:235], v[36:39], v[56:59]\n"
"v_rcp_f32 v27, v27\n"
"v_mfma_f32_16x16x32_bf16 v[60:63], a[236:239], v[36:39], v[60:63]\n"
"s_waitcnt lgkmcnt(2)\n"
"v_mfma_f32_16x16x32_bf16 v[72:75], a[240:243], v[40:43], v[72:75]\n"
"ds_read_b128 v[36:39], v0 offset:6208\n"
"v_mfma_f32_16x16x32_bf16 v[76:79], a[244:247], v[40:43], v[76:79]\n"
"v_rcp_f32 v28, v28\n"
"v_mfma_f32_16x16x32_bf16 v[64:67], a[248:251], v[40:43], v[64:67]\n"
"v_rcp_f32 v29, v29\n"
"v_mfma_f32_16x16x32_bf16 v[68:71], a[252:255], v[40:43], v[68:71]\n"
"v_rcp_f32 v30, v30\n"
"v_mfma_f32_16x16x32_bf16 v[56:59], v[128:131], v[40:43], v[56:59]\n"
"v_rcp_f32 v31, v31\n"
"v_mfma_f32_16x16x32_bf16 v[60:63], v[132:135], v[40:43], v[60:63]\n"
"v_mul_f32 v112, v112, v24\n"
"s_waitcnt lgkmcnt(2)\n"
"v_mfma_f32_16x16x32_bf16 v[72:75], v[136:139], v[44:47], v[72:75]\n"
"ds_read_b128 v[40:43], v0 offset:7168\n"
"v_mul_f32 v113, v113, v25\n"
"v_mfma_f32_16x16x32_bf16 v[76:79], v[140:143], v[44:47], v[76:79]\n"
"v_mul_f32 v114, v114, v26\n"
"v_mul_f32 v115, v115, v27\n"
"v_mfma_f32_16x16x32_bf16 v[64:67], v[144:147], v[44:47], v[64:67]\n"
"v_mul_f32 v116, v116, v28\n"
"v_mul_f32 v117, v117, v29\n"
"v_mfma_f32_16x16x32_bf16 v[68:71], v[148:151], v[44:47], v[68:71]\n"
"v_mul_f32 v118, v118, v30\n"
"v_mul_f32 v119, v119, v31\n"
"v_mfma_f32_16x16x32_bf16 v[56:59], v[152:155], v[44:47], v[56:59]\n"
"v_cvt_pk_bf16_f32 v112, v112, v113\n"
"v_cvt_pk_bf16_f32 v113, v114, v115\n"
"v_mfma_f32_16x16x32_bf16 v[60:63], v[156:159], v[44:47], v[60:63]\n"
"s_waitcnt vmcnt(12)\n"
"s_barrier\n"
"s_waitcnt lgkmcnt(2)\n"
"v_mfma_f32_16x16x32_bf16 v[72:75], v[160:163], v[32:35], v[72:75]\n"
"ds_read_b128 v[44:47], v0 offset:7232\n"
"v_mfma_f32_16x16x32_bf16 v[76:79], v[164:167], v[32:35], v[76:79]\n"
"v_add_u32 v0, s70, v1\n"
"v_cvt_pk_bf16_f32 v114, v116, v117\n"
"v_mfma_f32_16x16x32_bf16 v[64:67], v[168:171], v[32:35], v[64:67]\n"
"v_cvt_pk_bf16_f32 v115, v118, v119\n"
"v_mfma_f32_16x16x32_bf16 v[68:71], v[172:175], v[32:35], v[68:71]\n"
"buffer_store_dwordx4 v[112:115], v3, s[56:59], s82 offen offset:0 sc1 nt\n"
"v_mfma_f32_16x16x32_bf16 v[56:59], v[176:179], v[32:35], v[56:59]\n"
"v_fma_f32 v120, v120, v8, v16\n"
"v_fma_f32 v121, v121, v9, v17\n"
"v_mfma_f32_16x16x32_bf16 v[60:63], v[180:183], v[32:35], v[60:63]\n"
"v_fma_f32 v122, v122, v10, v18\n"
"s_waitcnt lgkmcnt(2)\n"
"v_mfma_f32_16x16x32_bf16 v[72:75], v[184:187], v[36:39], v[72:75]\n"
"ds_read_b128 v[32:35], v0 offset:0\n"
"v_fma_f32 v123, v123, v11, v19\n"
"v_mfma_f32_16x16x32_bf16 v[76:79], v[188:191], v[36:39], v[76:79]\n"
"v_fma_f32 v124, v124, v12, v20\n"
"v_mfma_f32_16x16x32_bf16 v[64:67], v[192:195], v[36:39], v[64:67]\n"
"v_fma_f32 v125, v125, v13, v21\n"
"v_fma_f32 v126, v126, v14, v22\n"
"v_mfma_f32_16x16x32_bf16 v[68:71], v[196:199], v[36:39], v[68:71]\n"
"v_fma_f32 v127, v127, v15, v23\n"
"v_mfma_f32_16x16x32_bf16 v[56:59], v[200:203], v[36:39], v[56:59]\n"
"v_exp_f32 v24, v120\n"
"v_mfma_f32_16x16x32_bf16 v[60:63], v[204:207], v[36:39], v[60:63]\n"
"s_waitcnt lgkmcnt(2)\n"
"v_mfma_f32_16x16x32_bf16 v[72:75], v[208:211], v[40:43], v[72:75]\n"
"ds_read_b128 v[36:39], v0 offset:64\n"
"v_mfma_f32_16x16x32_bf16 v[76:79], v[212:215], v[40:43], v[76:79]\n"
"v_exp_f32 v25, v121\n"
"v_mfma_f32_16x16x32_bf16 v[64:67], v[216:219], v[40:43], v[64:67]\n"
"v_exp_f32 v26, v122\n"
"v_mfma_f32_16x16x32_bf16 v[68:71], v[220:223], v[40:43], v[68:71]\n"
"v_exp_f32 v27, v123\n"
"v_mfma_f32_16x16x32_bf16 v[56:59], v[224:227], v[40:43], v[56:59]\n"
"v_exp_f32 v28, v124\n"
"v_mfma_f32_16x16x32_bf16 v[60:63], v[228:231], v[40:43], v[60:63]\n"
"s_waitcnt lgkmcnt(2)\n"
"v_mfma_f32_16x16x32_bf16 v[72:75], v[232:235], v[44:47], v[72:75]\n"
"ds_read_b128 v[40:43], v0 offset:1024\n"
"v_mfma_f32_16x16x32_bf16 v[76:79], v[236:239], v[44:47], v[76:79]\n"
"v_exp_f32 v29, v125\n"
"v_mfma_f32_16x16x32_bf16 v[64:67], v[240:243], v[44:47], v[64:67]\n"
"v_exp_f32 v30, v126\n"
"v_mfma_f32_16x16x32_bf16 v[68:71], v[244:247], v[44:47], v[68:71]\n"
"v_exp_f32 v31, v127\n"
"v_mfma_f32_16x16x32_bf16 v[56:59], v[248:251], v[44:47], v[56:59]\n"
"v_fma_f32 v24, v24, s83, s83\n"
"v_fma_f32 v25, v25, s83, s83\n"
"v_mfma_f32_16x16x32_bf16 v[60:63], v[252:255], v[44:47], v[60:63]\n"
"v_fma_f32 v26, v26, s83, s83\n"
"s_waitcnt lgkmcnt(2)\n"
"v_mfma_f32_16x16x32_bf16 v[80:83], a[0:3], v[32:35], 0\n"
"ds_read_b128 v[44:47], v0 offset:1088\n"
"v_fma_f32 v27, v27, s83, s83\n"
"v_mfma_f32_16x16x32_bf16 v[84:87], a[4:7], v[32:35], 0\n"
"s_add_u32 s70, s70, 16448\n"
"v_fma_f32 v28, v28, s83, s83\n"
"v_mfma_f32_16x16x32_bf16 v[72:75], a[8:11], v[32:35], v[72:75]\n"
"v_fma_f32 v29, v29, s83, s83\n"
"v_mfma_f32_16x16x32_bf16 v[76:79], a[12:15], v[32:35], v[76:79]\n"
"s_cmp_eq_u32 s70, 148032\ns_cselect_b32 s70, 0, s70\n"
"v_mfma_f32_16x16x32_bf16 v[64:67], a[16:19], v[32:35], v[64:67]\n"
"v_fma_f32 v30, v30, s83, s83\n"
"v_fma_f32 v31, v31, s83, s83\n"
"v_mfma_f32_16x16x32_bf16 v[68:71], a[20:23], v[32:35], v[68:71]\n"
"s_waitcnt lgkmcnt(2)\n"
"v_mfma_f32_16x16x32_bf16 v[80:83], a[24:27], v[36:39], v[80:83]\n"
"ds_read_b128 v[32:35], v0 offset:2048\n"
"v_mfma_f32_16x16x32_bf16 v[84:87], a[28:31], v[36:39], v[84:87]\n"
"v_rcp_f32 v24, v24\n"
"v_mfma_f32_16x16x32_bf16 v[72:75], a[32:35], v[36:39], v[72:75]\n"
"s_mov_b32 m0, s68\n"
"v_mfma_f32_16x16x32_bf16 v[76:79], a[36:39], v[36:39], v[76:79]\n"
"v_rcp_f32 v25, v25\n"
"v_mfma_f32_16x16x32_bf16 v[64:67], a[40:43], v[36:39], v[64:67]\n"
"v_rcp_f32 v26, v26\n"
"v_mfma_f32_16x16x32_bf16 v[68:71], a[44:47], v[36:39], v[68:71]\n"
"s_waitcnt lgkmcnt(2)\n"
"v_mfma_f32_16x16x32_bf16 v[80:83], a[48:51], v[40:43], v[80:83]\n"
"ds_read_b128 v[36:39], v0 offset:2112\n"
"v_mfma_f32_16x16x32_bf16 v[84:87], a[52:55], v[40:43], v[84:87]\n"
"buffer_load_dwordx4 v2, s[36:39], s79 offen nt lds\n"
"v_mfma_f32_16x16x32_bf16 v[72:75], a[56:59], v[40:43], v[72:75]\n"
"v_rcp_f32 v27, v27\n"
"v_mfma_f32_16x16x32_bf16 v[76:79], a[60:63], v[40:43], v[76:79]\n"
"v_rcp_f32 v28, v28\n"
"v_mfma_f32_16x16x32_bf16 v[64:67], a[64:67], v[40:43], v[64:67]\n"
"s_add_u32 m0, s68, 8256\n"
"v_mfma_f32_16x16x32_bf16 v[68:71], a[68:71], v[40:43], v[68:71]\n"
"s_waitcnt lgkmcnt(2)\n"
"v_mfma_f32_16x16x32_bf16 v[80:83], a[72:75], v[44:47], v[80:83]\n"
"ds_read_b128 v[40:43], v0 offset:3072\n"
"v_mfma_f32_16x16x32_bf16 v[84:87], a[76:79], v[44:47], v[84:87]\n"
"buffer_load_dwordx4 v2, s[40:43], s79 offen nt lds\n"
"v_mfma_f32_16x16x32_bf16 v[72:75], a[80:83], v[44:47], v[72:75]\n"
"v_rcp_f32 v29, v29\n"
"v_mfma_f32_16x16x32_bf16 v[76:79], a[84:87], v[44:47], v[76:79]\n"
"v_rcp_f32 v30, v30\n"
"v_mfma_f32_16x16x32_bf16 v[64:67], a[88:91], v[44:47], v[64:67]\n"
"v_rcp_f32 v31, v31\n"
"v_mfma_f32_16x16x32_bf16 v[68:71], a[92:95], v[44:47], v[68:71]\n"
"v_mul_f32 v120, v120, v24\n"
"s_waitcnt lgkmcnt(2)\n"
"v_mfma_f32_16x16x32_bf16 v[80:83], a[96:99], v[32:35], v[80:83]\n"
"ds_read_b128 v[44:47], v0 offset:3136\n"
"v_mul_f32 v121, v121, v25\n"
"v_mfma_f32_16x16x32_bf16 v[84:87], a[100:103], v[32:35], v[84:87]\n"
"s_add_u32 s79, s79, 1024\n"
"v_mul_f32 v122, v122, v26\n"
"v_mfma_f32_16x16x32_bf16 v[72:75], a[104:107], v[32:35], v[72:75]\n"
"v_mul_f32 v123, v123, v27\n"
"v_mul_f32 v124, v124, v28\n"
"v_mfma_f32_16x16x32_bf16 v[76:79], a[108:111], v[32:35], v[76:79]\n"
"s_add_u32 m0, s68, 1024\n"
"v_mul_f32 v125, v125, v29\n"
"v_mfma_f32_16x16x32_bf16 v[64:67], a[112:115], v[32:35], v[64:67]\n"
"v_mul_f32 v126, v126, v30\n"
"v_mul_f32 v127, v127, v31\n"
"v_mfma_f32_16x16x32_bf16 v[68:71], a[116:119], v[32:35], v[68:71]\n"
"v_cvt_pk_bf16_f32 v120, v120, v121\n"
"s_waitcnt lgkmcnt(2)\n"
"v_mfma_f32_16x16x32_bf16 v[80:83], a[120:123], v[36:39], v[80:83]\n"
"ds_read_b128 v[32:35], v0 offset:4096\n"
"v_cvt_pk_bf16_f32 v121, v122, v123\n"
"v_mfma_f32_16x16x32_bf16 v[84:87], a[124:127], v[36:39], v[84:87]\n"
"buffer_load_dwordx4 v2, s[36:39], s80 offen nt lds\n"
"v_mfma_f32_16x16x32_bf16 v[72:75], a[128:131], v[36:39], v[72:75]\n"
"v_cvt_pk_bf16_f32 v122, v124, v125\n"
"v_cvt_pk_bf16_f32 v123, v126, v127\n"
"v_mfma_f32_16x16x32_bf16 v[76:79], a[132:135], v[36:39], v[76:79]\n"
"buffer_store_dwordx4 v[120:123], v3, s[56:59], s82 offen offset:1024 sc1 nt\n"
"v_mfma_f32_16x16x32_bf16 v[64:67], a[136:139], v[36:39], v[64:67]\n"
"v_fma_f32 v48, v48, v8, v16\n"
"v_fma_f32 v49, v49, v9, v17\n"
"v_mfma_f32_16x16x32_bf16 v[68:71], a[140:143], v[36:39], v[68:71]\n"
"v_fma_f32 v50, v50, v10, v18\n"
"s_waitcnt lgkmcnt(2)\n"
"v_mfma_f32_16x16x32_bf16 v[80:83], a[144:147], v[40:43], v[80:83]\n"
"ds_read_b128 v[36:39], v0 offset:4160\n"
"s_add_u32 m0, s68, 9280\n"
"v_mfma_f32_16x16x32_bf16 v[84:87], a[148:151], v[40:43], v[84:87]\n"
"v_fma_f32 v51, v51, v11, v19\n"
"v_mfma_f32_16x16x32_bf16 v[72:75], a[152:155], v[40:43], v[72:75]\n"
"buffer_load_dwordx4 v2, s[40:43], s80 offen nt lds\n"
"v_mfma_f32_16x16x32_bf16 v[76:79], a[156:159], v[40:43], v[76:79]\n"
"v_fma_f32 v52, v52, v12, v20\n"
"v_fma_f32 v53, v53, v13, v21\n"
"v_mfma_f32_16x16x32_bf16 v[64:67], a[160:163], v[40:43], v[64:67]\n"
"v_fma_f32 v54, v54, v14, v22\n"
"v_fma_f32 v55, v55, v15, v23\n"
"v_mfma_f32_16x16x32_bf16 v[68:71], a[164:167], v[40:43], v[68:71]\n"
"s_waitcnt lgkmcnt(2)\n"
"v_mfma_f32_16x16x32_bf16 v[80:83], a[168:171], v[44:47], v[80:83]\n"
"ds_read_b128 v[40:43], v0 offset:5120\n"
"v_mfma_f32_16x16x32_bf16 v[84:87], a[172:175], v[44:47], v[84:87]\n"
"v_exp_f32 v24, v48\n"
"v_mfma_f32_16x16x32_bf16 v[72:75], a[176:179], v[44:47], v[72:75]\n"
"s_add_u32 s80, s80, 1024\n"
"v_mfma_f32_16x16x32_bf16 v[76:79], a[180:183], v[44:47], v[76:79]\n"
"v_exp_f32 v25, v49\n"
"v_mfma_f32_16x16x32_bf16 v[64:67], a[184:187], v[44:47], v[64:67]\n"
"s_add_u32 s68, s68, 16448\n"
"v_mfma_f32_16x16x32_bf16 v[68:71], a[188:191], v[44:47], v[68:71]\n"
"s_waitcnt lgkmcnt(2)\n"
"v_mfma_f32_16x16x32_bf16 v[80:83], a[192:195], v[32:35], v[80:83]\n"
"ds_read_b128 v[44:47], v0 offset:5184\n"
"v_mfma_f32_16x16x32_bf16 v[84:87], a[196:199], v[32:35], v[84:87]\n"
"s_cmp_eq_u32 s68, s69\ns_cselect_b32 s68, s81, s68\n"
"v_mfma_f32_16x16x32_bf16 v[72:75], a[200:203], v[32:35], v[72:75]\n"
"v_exp_f32 v26, v50\n"
"v_mfma_f32_16x16x32_bf16 v[76:79], a[204:207], v[32:35], v[76:79]\n"
"v_exp_f32 v27, v51\n"
"v_mfma_f32_16x16x32_bf16 v[64:67], a[208:211], v[32:35], v[64:67]\n"
"v_exp_f32 v28, v52\n"
"v_mfma_f32_16x16x32_bf16 v[68:71], a[212:215], v[32:35], v[68:71]\n"
"s_waitcnt lgkmcnt(2)\n"
"v_mfma_f32_16x16x32_bf16 v[80:83], a[216:219], v[36:39], v[80:83]\n"
"ds_read_b128 v[32:35], v0 offset:6144\n"
"v_mfma_f32_16x16x32_bf16 v[84:87], a[220:223], v[36:39], v[84:87]\n"
"v_exp_f32 v29, v53\n"
"v_mfma_f32_16x16x32_bf16 v[72:75], a[224:227], v[36:39], v[72:75]\n"
"v_exp_f32 v30, v54\n"
"v_mfma_f32_16x16x32_bf16 v[76:79], a[228:231], v[36:39], v[76:79]\n"
"v_exp_f32 v31, v55\n"
"v_mfma_f32_16x16x32_bf16 v[64:67], a[232:235], v[36:39], v[64:67]\n"
"v_fma_f32 v24, v24, s83, s83\n"
"v_fma_f32 v25, v25, s83, s83\n"
"v_mfma_f32_16x16x32_bf16 v[68:71], a[236:239], v[36:39], v[68:71]\n"
"v_fma_f32 v26, v26, s83, s83\n"
"s_waitcnt lgkmcnt(2)\n"
"v_mfma_f32_16x16x32_bf16 v[80:83], a[240:243], v[40:43], v[80:83]\n"
"ds_read_b128 v[36:39], v0 offset:6208\n"
"v_fma_f32 v27, v27, s83, s83\n"
"v_mfma_f32_16x16x32_bf16 v[84:87], a[244:247], v[40:43], v[84:87]\n"
"v_fma_f32 v28, v28, s83, s83\n"
"v_fma_f32 v29, v29, s83, s83\n"
"v_mfma_f32_16x16x32_bf16 v[72:75], a[248:251], v[40:43], v[72:75]\n"
"v_fma_f32 v30, v30, s83, s83\n"
"v_fma_f32 v31, v31, s83, s83\n"
"v_mfma_f32_16x16x32_bf16 v[76:79], a[252:255], v[40:43], v[76:79]\n"
"v_rcp_f32 v24, v24\n"
"v_mfma_f32_16x16x32_bf16 v[64:67], v[128:131], v[40:43], v[64:67]\n"
"v_rcp_f32 v25, v25\n"
"v_mfma_f32_16x16x32_bf16 v[68:71], v[132:135], v[40:43], v[68:71]\n"
"s_waitcnt lgkmcnt(2)\n"
"v_mfma_f32_16x16x32_bf16 v[80:83], v[136:139], v[44:47], v[80:83]\n"
"ds_read_b128 v[40:43], v0 offset:7168\n"
"v_mfma_f32_16x16x32_bf16 v[84:87], v[140:143], v[44:47], v[84:87]\n"
"v_rcp_f32 v26, v26\n"
"v_mfma_f32_16x16x32_bf16 v[72:75], v[144:147], v[44:47], v[72:75]\n"
"v_rcp_f32 v27, v27\n"
"v_mfma_f32_16x16x32_bf16 v[76:79], v[148:151], v[44:47], v[76:79]\n"
"v_rcp_f32 v28, v28\n"
"v_mfma_f32_16x16x32_bf16 v[64:67], v[152:155], v[44:47], v[64:67]\n"
"v_rcp_f32 v29, v29\n"
"v_mfma_f32_16x16x32_bf16 v[68:71], v[156:159], v[44:47], v[68:71]\n"
"s_waitcnt lgkmcnt(2)\n"
"v_mfma_f32_16x16x32_bf16 v[80:83], v[160:163], v[32:35], v[80:83]\n"
"ds_read_b128 v[44:47], v0 offset:7232\n"
"v_mfma_f32_16x16x32_bf16 v[84:87], v[164:167], v[32:35], v[84:87]\n"
"v_add_u32 v0, s70, v1\n"
"v_mfma_f32_16x16x32_bf16 v[72:75], v[168:171], v[32:35], v[72:75]\n"
"v_rcp_f32 v30, v30\n"
"v_mfma_f32_16x16x32_bf16 v[76:79], v[172:175], v[32:35], v[76:79]\n"
"v_rcp_f32 v31, v31\n"
"v_mfma_f32_16x16x32_bf16 v[64:67], v[176:179], v[32:35], v[64:67]\n"
"v_mul_f32 v48, v48, v24\n"
"v_mul_f32 v49, v49, v25\n"
"v_mfma_f32_16x16x32_bf16 v[68:71], v[180:183], v[32:35], v[68:71]\n"
"v_mul_f32 v50, v50, v26\n"
"s_waitcnt lgkmcnt(2)\n"
"v_mfma_f32_16x16x32_bf16 v[80:83], v[184:187], v[36:39], v[80:83]\n"
"ds_read_b128 v[32:35], v0 offset:0\n"
"v_mul_f32 v51, v51, v27\n"
"v_mfma_f32_16x16x32_bf16 v[84:87], v[188:191], v[36:39], v[84:87]\n"
"v_mul_f32 v52, v52, v28\n"
"v_mul_f32 v53, v53, v29\n"
"v_mfma_f32_16x16x32_bf16 v[72:75], v[192:195], v[36:39], v[72:75]\n"
"v_mul_f32 v54, v54, v30\n"
"v_mul_f32 v55, v55, v31\n"
"v_mfma_f32_16x16x32_bf16 v[76:79], v[196:199], v[36:39], v[76:79]\n"
"v_cvt_pk_bf16_f32 v48, v48, v49\n"
"v_cvt_pk_bf16_f32 v49, v50, v51\n"
"v_mfma_f32_16x16x32_bf16 v[64:67], v[200:203], v[36:39], v[64:67]\n"
"v_cvt_pk_bf16_f32 v50, v52, v53\n"
"v_mfma_f32_16x16x32_bf16 v[68:71], v[204:207], v[36:39], v[68:71]\n"
"v_cvt_pk_bf16_f32 v51, v54, v55\n"
"s_waitcnt lgkmcnt(2)\n"
"v_mfma_f32_16x16x32_bf16 v[80:83], v[208:211], v[40:43], v[80:83]\n"
"ds_read_b128 v[36:39], v0 offset:64\n"
"v_mfma_f32_16x16x32_bf16 v[84:87], v[212:215], v[40:43], v[84:87]\n"
"buffer_store_dwordx4 v[48:51], v3, s[56:59], s82 offen offset:2048 sc1 nt\n"
"v_mfma_f32_16x16x32_bf16 v[72:75], v[216:219], v[40:43], v[72:75]\n"
"v_fma_f32 v56, v56, v8, v16\n"
"v_fma_f32 v57, v57, v9, v17\n"
"v_mfma_f32_16x16x32_bf16 v[76:79], v[220:223], v[40:43], v[76:79]\n"
"v_fma_f32 v58, v58, v10, v18\n"
"v_fma_f32 v59, v59, v11, v19\n"
"v_mfma_f32_16x16x32_bf16 v[64:67], v[224:227], v[40:43], v[64:67]\n"
"v_fma_f32 v60, v60, v12, v20\n"
"v_mfma_f32_16x16x32_bf16 v[68:71], v[228:231], v[40:43], v[68:71]\n"
"v_fma_f32 v61, v61, v13, v21\n"
"s_waitcnt lgkmcnt(2)\n"
"v_mfma_f32_16x16x32_bf16 v[80:83], v[232:235], v[44:47], v[80:83]\n"
"ds_read_b128 v[40:43], v0 offset:1024\n"
"v_fma_f32 v62, v62, v14, v22\n"
"v_mfma_f32_16x16x32_bf16 v[84:87], v[236:239], v[44:47], v[84:87]\n"
"v_fma_f32 v63, v63, v15, v23\n"
"v_mfma_f32_16x16x32_bf16 v[72:75], v[240:243], v[44:47], v[72:75]\n"
"v_exp_f32 v24, v56\n"
"v_mfma_f32_16x16x32_bf16 v[76:79], v[244:247], v[44:47], v[76:79]\n"
"v_exp_f32 v25, v57\n"
"v_mfma_f32_16x16x32_bf16 v[64:67], v[248:251], v[44:47], v[64:67]\n"
"v_exp_f32 v26, v58\n"
"v_mfma_f32_16x16x32_bf16 v[68:71], v[252:255], v[44:47], v[68:71]\n"
"s_waitcnt lgkmcnt(2)\n"
"v_mfma_f32_16x16x32_bf16 v[88:91], a[0:3], v[32:35], 0\n"
"ds_read_b128 v[44:47], v0 offset:1088\n"
"v_mfma_f32_16x16x32_bf16 v[92:95], a[4:7], v[32:35], 0\n"
"s_add_u32 s70, s70, 16448\n"
"v_mfma_f32_16x16x32_bf16 v[80:83], a[8:11], v[32:35], v[80:83]\n"
"v_exp_f32 v27, v59\n"
"v_mfma_f32_16x16x32_bf16 v[84:87], a[12:15], v[32:35], v[84:87]\n"
"s_cmp_eq_u32 s70, 148032\ns_cselect_b32 s70, 0, s70\n"
"v_mfma_f32_16x16x32_bf16 v[72:75], a[16:19], v[32:35], v[72:75]\n"
"v_exp_f32 v28, v60\n"
"v_mfma_f32_16x16x32_bf16 v[76:79], a[20:23], v[32:35], v[76:79]\n"
"s_waitcnt lgkmcnt(2)\n"
"v_mfma_f32_16x16x32_bf16 v[88:91], a[24:27], v[36:39], v[88:91]\n"
"ds_read_b128 v[32:35], v0 offset:2048\n"
"v_mfma_f32_16x16x32_bf16 v[92:95], a[28:31], v[36:39], v[92:95]\n"
"v_exp_f32 v29, v61\n"
"v_mfma_f32_16x16x32_bf16 v[80:83], a[32:35], v[36:39], v[80:83]\n"
"s_mov_b32 m0, s68\n"
"v_mfma_f32_16x16x32_bf16 v[84:87], a[36:39], v[36:39], v[84:87]\n"
"v_exp_f32 v30, v62\n"
"v_mfma_f32_16x16x32_bf16 v[72:75], a[40:43], v[36:39], v[72:75]\n"
"v_exp_f32 v31, v63\n"
"v_mfma_f32_16x16x32_bf16 v[76:79], a[44:47], v[36:39], v[76:79]\n"
"v_fma_f32 v24, v24, s83, s83\n"
"s_waitcnt lgkmcnt(2)\n"
"v_mfma_f32_16x16x32_bf16 v[88:91], a[48:51], v[40:43], v[88:91]\n"
"ds_read_b128 v[36:39], v0 offset:2112\n"
"v_fma_f32 v25, v25, s83, s83\n"
"v_mfma_f32_16x16x32_bf16 v[92:95], a[52:55], v[40:43], v[92:95]\n"
"buffer_load_dwordx4 v2, s[36:39], s79 offen nt lds\n"
"v_mfma_f32_16x16x32_bf16 v[80:83], a[56:59], v[40:43], v[80:83]\n"
"v_fma_f32 v26, v26, s83, s83\n"
"v_fma_f32 v27, v27, s83, s83\n"
"v_mfma_f32_16x16x32_bf16 v[84:87], a[60:63], v[40:43], v[84:87]\n"
"v_fma_f32 v28, v28, s83, s83\n"
"v_fma_f32 v29, v29, s83, s83\n"
"v_mfma_f32_16x16x32_bf16 v[72:75], a[64:67], v[40:43], v[72:75]\n"
"s_add_u32 m0, s68, 8256\n"
"v_fma_f32 v30, v30, s83, s83\n"
"v_mfma_f32_16x16x32_bf16 v[76:79], a[68:71], v[40:43], v[76:79]\n"
"v_fma_f32 v31, v31, s83, s83\n"
"s_waitcnt lgkmcnt(2)\n"
"v_mfma_f32_16x16x32_bf16 v[88:91], a[72:75], v[44:47], v[88:91]\n"
"ds_read_b128 v[40:43], v0 offset:3072\n"
"v_mfma_f32_16x16x32_bf16 v[92:95], a[76:79], v[44:47], v[92:95]\n"
"buffer_load_dwordx4 v2, s[40:43], s79 offen nt lds\n"
"v_mfma_f32_16x16x32_bf16 v[80:83], a[80:83], v[44:47], v[80:83]\n"
"v_rcp_f32 v24, v24\n"
"v_mfma_f32_16x16x32_bf16 v[84:87], a[84:87], v[44:47], v[84:87]\n"
"v_rcp_f32 v25, v25\n"
"v_mfma_f32_16x16x32_bf16 v[72:75], a[88:91], v[44:47], v[72:75]\n"
"v_rcp_f32 v26, v26\n"
"v_mfma_f32_16x16x32_bf16 v[76:79], a[92:95], v[44:47], v[76:79]\n"
"s_waitcnt lgkmcnt(2)\n"
"v_mfma_f32_16x16x32_bf16 v[88:91], a[96:99], v[32:35], v[88:91]\n"
"ds_read_b128 v[44:47], v0 offset:3136\n"
"v_mfma_f32_16x16x32_bf16 v[92:95], a[100:103], v[32:35], v[92:95]\n"
"s_add_u32 s79, s79, 1024\n"
"v_mfma_f32_16x16x32_bf16 v[80:83], a[104:107], v[32:35], v[80:83]\n"
"v_rcp_f32 v27, v27\n"
"v_mfma_f32_16x16x32_bf16 v[84:87], a[108:111], v[32:35], v[84:87]\n"
"s_add_u32 m0, s68, 1024\n"
"v_mfma_f32_16x16x32_bf16 v[72:75], a[112:115], v[32:35], v[72:75]\n"
"v_rcp_f32 v28, v28\n"
"v_mfma_f32_16x16x32_bf16 v[76:79], a[116:119], v[32:35], v[76:79]\n"
"s_waitcnt lgkmcnt(2)\n"
"v_mfma_f32_16x16x32_bf16 v[88:91], a[120:123], v[36:39], v[88:91]\n"
"ds_read_b128 v[32:35], v0 offset:4096\n"
"v_mfma_f32_16x16x32_bf16 v[92:95], a[124:127], v[36:39], v[92:95]\n"
"buffer_load_dwordx4 v2, s[36:39], s80 offen nt lds\n"
"v_mfma_f32_16x16x32_bf16 v[80:83], a[128:131], v[36:39], v[80:83]\n"
"v_rcp_f32 v29, v29\n"
"v_mfma_f32_16x16x32_bf16 v[84:87], a[132:135], v[36:39], v[84:87]\n"
"v_rcp_f32 v30, v30\n"
"v_mfma_f32_16x16x32_bf16 v[72:75], a[136:139], v[36:39], v[72:75]\n"
"v_rcp_f32 v31, v31\n"
"v_mfma_f32_16x16x32_bf16 v[76:79], a[140:143], v[36:39], v[76:79]\n"
"v_mul_f32 v56, v56, v24\n"
"s_waitcnt lgkmcnt(2)\n"
"v_mfma_f32_16x16x32_bf16 v[88:91], a[144:147], v[40:43], v[88:91]\n"
"ds_read_b128 v[36:39], v0 offset:4160\n"
"s_add_u32 m0, s68, 9280\n"
"v_mfma_f32_16x16x32_bf16 v[92:95], a[148:151], v[40:43], v[92:95]\n"
"v_mul_f32 v57, v57, v25\n"
"v_mul_f32 v58, v58, v26\n"
"v_mfma_f32_16x16x32_bf16 v[80:83], a[152:155], v[40:43], v[80:83]\n"
"buffer_load_dwordx4 v2, s[40:43], s80 offen nt lds\n"
"v_mfma_f32_16x16x32_bf16 v[84:87], a[156:159], v[40:43], v[84:87]\n"
"v_mul_f32 v59, v59, v27\n"
"v_mul_f32 v60, v60, v28\n"
"v_mfma_f32_16x16x32_bf16 v[72:75], a[160:163], v[40:43], v[72:75]\n"
"v_mul_f32 v61, v61, v29\n"
"v_mul_f32 v62, v62, v30\n"
"v_mfma_f32_16x16x32_bf16 v[76:79], a[164:167], v[40:43], v[76:79]\n"
"v_mul_f32 v63, v63, v31\n"
"s_waitcnt lgkmcnt(2)\n"
"v_mfma_f32_16x16x32_bf16 v[88:91], a[168:171], v[44:47], v[88:91]\n"
"ds_read_b128 v[40:43], v0 offset:5120\n"
"v_cvt_pk_bf16_f32 v56, v56, v57\n"
"v_mfma_f32_16x16x32_bf16 v[92:95], a[172:175], v[44:47], v[92:95]\n"
"v_cvt_pk_bf16_f32 v57, v58, v59\n"
"v_cvt_pk_bf16_f32 v58, v60, v61\n"
"v_mfma_f32_16x16x32_bf16 v[80:83], a[176:179], v[44:47], v[80:83]\n"
"s_add_u32 s80, s80, 1024\n"
"v_cvt_pk_bf16_f32 v59, v62, v63\n"
"v_mfma_f32_16x16x32_bf16 v[84:87], a[180:183], v[44:47], v[84:87]\n"
"buffer_store_dwordx4 v[56:59], v3, s[56:59], s82 offen offset:3072 sc1 nt\n"
"s_add_u32 s82, s82, 4096\n"
"v_mfma_f32_16x16x32_bf16 v[72:75], a[184:187], v[44:47], v[72:75]\n"
"s_add_u32 s68, s68, 16448\n"
"v_fma_f32 v64, v64, v8, v16\n"
"v_mfma_f32_16x16x32_bf16 v[76:79], a[188:191], v[44:47], v[76:79]\n"
"v_fma_f32 v65, v65, v9, v17\n"
"s_waitcnt lgkmcnt(2)\n"
"v_mfma_f32_16x16x32_bf16 v[88:91], a[192:195], v[32:35], v[88:91]\n"
"ds_read_b128 v[44:47], v0 offset:5184\n"
"v_fma_f32 v66, v66, v10, v18\n"
"v_mfma_f32_16x16x32_bf16 v[92:95], a[196:199], v[32:35], v[92:95]\n"
"s_cmp_eq_u32 s68, s69\ns_cselect_b32 s68, s81, s68\n"
"v_mfma_f32_16x16x32_bf16 v[80:83], a[200:203], v[32:35], v[80:83]\n"
"v_fma_f32 v67, v67, v11, v19\n"
"v_fma_f32 v68, v68, v12, v20\n"
"v_mfma_f32_16x16x32_bf16 v[84:87], a[204:207], v[32:35], v[84:87]\n"
"v_fma_f32 v69, v69, v13, v21\n"
"v_mfma_f32_16x16x32_bf16 v[72:75], a[208:211], v[32:35], v[72:75]\n"
"v_fma_f32 v70, v70, v14, v22\n"
"v_fma_f32 v71, v71, v15, v23\n"
"v_mfma_f32_16x16x32_bf16 v[76:79], a[212:215], v[32:35], v[76:79]\n"
"s_waitcnt lgkmcnt(2)\n"
"v_mfma_f32_16x16x32_bf16 v[88:91], a[216:219], v[36:39], v[88:91]\n"
"ds_read_b128 v[32:35], v0 offset:6144\n"
"v_mfma_f32_16x16x32_bf16 v[92:95], a[220:223], v[36:39], v[92:95]\n"
"v_exp_f32 v24, v64\n"
"v_mfma_f32_16x16x32_bf16 v[80:83], a[224:227], v[36:39], v[80:83]\n"
"v_exp_f32 v25, v65\n"
"v_mfma_f32_16x16x32_bf16 v[84:87], a[228:231], v[36:39], v[84:87]\n"
"v_exp_f32 v26, v66\n"
"v_mfma_f32_16x16x32_bf16 v[72:75], a[232:235], v[36:39], v[72:75]\n"
"v_exp_f32 v27, v67\n"
"v_mfma_f32_16x16x32_bf16 v[76:79], a[236:239], v[36:39], v[76:79]\n"
"s_waitcnt lgkmcnt(2)\n"
"v_mfma_f32_16x16x32_bf16 v[88:91], a[240:243], v[40:43], v[88:91]\n"
"ds_read_b128 v[36:39], v0 offset:6208\n"
"v_mfma_f32_16x16x32_bf16 v[92:95], a[244:247], v[40:43], v[92:95]\n"
"v_exp_f32 v28, v68\n"
"v_mfma_f32_16x16x32_bf16 v[80:83], a[248:251], v[40:43], v[80:83]\n"
"v_exp_f32 v29, v69\n"
"v_mfma_f32_16x16x32_bf16 v[84:87], a[252:255], v[40:43], v[84:87]\n"
"v_exp_f32 v30, v70\n"
"v_mfma_f32_16x16x32_bf16 v[72:75], v[128:131], v[40:43], v[72:75]\n"
"v_exp_f32 v31, v71\n"
"v_mfma_f32_16x16x32_bf16 v[76:79], v[132:135], v[40:43], v[76:79]\n"
"v_fma_f32 v24, v24, s83, s83\n"
"s_waitcnt lgkmcnt(2)\n"
"v_mfma_f32_16x16x32_bf16 v[88:91], v[136:139], v[44:47], v[88:91]\n"
"ds_read_b128 v[40:43], v0 offset:7168\n"
"v_fma_f32 v25, v25, s83, s83\n"
"v_mfma_f32_16x16x32_bf16 v[92:95], v[140:143], v[44:47], v[92:95]\n"
"v_fma_f32 v26, v26, s83, s83\n"
"v_fma_f32 v27, v27, s83, s83\n"
"v_mfma_f32_16x16x32_bf16 v[80:83], v[144:147], v[44:47], v[80:83]\n"
"v_fma_f32 v28, v28, s83, s83\n"
"v_mfma_f32_16x16x32_bf16 v[84:87], v[148:151], v[44:47], v[84:87]\n"
"v_fma_f32 v29, v29, s83, s83\n"
"v_fma_f32 v30, v30, s83, s83\n"
"v_mfma_f32_16x16x32_bf16 v[72:75], v[152:155], v[44:47], v[72:75]\n"
"v_fma_f32 v31, v31, s83, s83\n"
"v_mfma_f32_16x16x32_bf16 v[76:79], v[156:159], v[44:47], v[76:79]\n"
"s_waitcnt vmcnt(12)\n"
"s_barrier\n"
"s_waitcnt lgkmcnt(2)\n"
"v_mfma_f32_16x16x32_bf16 v[88:91], v[160:163], v[32:35], v[88:91]\n"
"ds_read_b128 v[44:47], v0 offset:7232\n"
"v_mfma_f32_16x16x32_bf16 v[92:95], v[164:167], v[32:35], v[92:95]\n"
"v_add_u32 v0, s70, v1\n"
"v_mfma_f32_16x16x32_bf16 v[80:83], v[168:171], v[32:35], v[80:83]\n"
"v_rcp_f32 v24, v24\n"
"v_mfma_f32_16x16x32_bf16 v[84:87], v[172:175], v[32:35], v[84:87]\n"
"v_rcp_f32 v25, v25\n"
"v_mfma_f32_16x16x32_bf16 v[72:75], v[176:179], v[32:35], v[72:75]\n"
"v_rcp_f32 v26, v26\n"
"v_mfma_f32_16x16x32_bf16 v[76:79], v[180:183], v[32:35], v[76:79]\n"
"s_waitcnt lgkmcnt(2)\n"
"v_mfma_f32_16x16x32_bf16 v[88:91], v[184:187], v[36:39], v[88:91]\n"
"ds_read_b128 v[32:35], v0 offset:0\n"
"v_mfma_f32_16x16x32_bf16 v[92:95], v[188:191], v[36:39], v[92:95]\n"
"v_rcp_f32 v27, v27\n"
"v_mfma_f32_16x16x32_bf16 v[80:83], v[192:195], v[36:39], v[80:83]\n"
"v_rcp_f32 v28, v28\n"
"v_mfma_f32_16x16x32_bf16 v[84:87], v[196:199], v[36:39], v[84:87]\n"
"v_rcp_f32 v29, v29\n"
"v_mfma_f32_16x16x32_bf16 v[72:75], v[200:203], v[36:39], v[72:75]\n"
"v_rcp_f32 v30, v30\n"
"v_mfma_f32_16x16x32_bf16 v[76:79], v[204:207], v[36:39], v[76:79]\n"
"s_waitcnt lgkmcnt(2)\n"
"v_mfma_f32_16x16x32_bf16 v[88:91], v[208:211], v[40:43], v[88:91]\n"
"ds_read_b128 v[36:39], v0 offset:64\n"
"v_mfma_f32_16x16x32_bf16 v[92:95], v[212:215], v[40:43], v[92:95]\n"
"v_rcp_f32 v31, v31\n"
"v_mfma_f32_16x16x32_bf16 v[80:83], v[216:219], v[40:43], v[80:83]\n"
"v_mul_f32 v64, v64, v24\n"
"v_mul_f32 v65, v65, v25\n"
"v_mfma_f32_16x16x32_bf16 v[84:87], v[220:223], v[40:43], v[84:87]\n"
"v_mul_f32 v66, v66, v26\n"
"v_mul_f32 v67, v67, v27\n"
"v_mfma_f32_16x16x32_bf16 v[72:75], v[224:227], v[40:43], v[72:75]\n"
"v_mul_f32 v68, v68, v28\n"
"v_mul_f32 v69, v69, v29\n"
"v_mfma_f32_16x16x32_bf16 v[76:79], v[228:231], v[40:43], v[76:79]\n"
"v_mul_f32 v70, v70, v30\n"
"s_waitcnt lgkmcnt(2)\n"
"v_mfma_f32_16x16x32_bf16 v[88:91], v[232:235], v[44:47], v[88:91]\n"
"ds_read_b128 v[40:43], v0 offset:1024\n"
"v_mul_f32 v71, v71, v31\n"
"v_mfma_f32_16x16x32_bf16 v[92:95], v[236:239], v[44:47], v[92:95]\n"
"v_cvt_pk_bf16_f32 v64, v64, v65\n"
"v_cvt_pk_bf16_f32 v65, v66, v67\n"
"v_mfma_f32_16x16x32_bf16 v[80:83], v[240:243], v[44:47], v[80:83]\n"
"v_cvt_pk_bf16_f32 v66, v68, v69\n"
"v_cvt_pk_bf16_f32 v67, v70, v71\n"
"v_mfma_f32_16x16x32_bf16 v[84:87], v[244:247], v[44:47], v[84:87]\n"
"buffer_store_dwordx4 v[64:67], v3, s[56:59], s82 offen offset:0 sc1 nt\n"
"v_mfma_f32_16x16x32_bf16 v[72:75], v[248:251], v[44:47], v[72:75]\n"
"v_mfma_f32_16x16x32_bf16 v[76:79], v[252:255], v[44:47], v[76:79]\n"
"Lloop_%=:\n"
"s_waitcnt lgkmcnt(2)\n"
"v_mfma_f32_16x16x32_bf16 v[96:99], a[0:3], v[32:35], 0\n"
"ds_read_b128 v[44:47], v0 offset:1088\n"
"v_mfma_f32_16x16x32_bf16 v[100:103], a[4:7], v[32:35], 0\n"
"s_cmp_eq_u32 s71, 1\ns_cselect_b32 s38, 0, s38\ns_cselect_b32 s42, 0, s42\n"
"v_mfma_f32_16x16x32_bf16 v[88:91], a[8:11], v[32:35], v[88:91]\n"
"v_mfma_f32_16x16x32_bf16 v[92:95], a[12:15], v[32:35], v[92:95]\n"
"v_mfma_f32_16x16x32_bf16 v[80:83], a[16:19], v[32:35], v[80:83]\n"
"v_mfma_f32_16x16x32_bf16 v[84:87], a[20:23], v[32:35], v[84:87]\n"
"s_waitcnt lgkmcnt(2)\n"
"v_mfma_f32_16x16x32_bf16 v[96:99], a[24:27], v[36:39], v[96:99]\n"
"ds_read_b128 v[32:35], v0 offset:2048\n"
"v_fma_f32 v72, v72, v8, v16\n"
"v_mfma_f32_16x16x32_bf16 v[100:103], a[28:31], v[36:39], v[100:103]\n"
"s_add_u32 s70, s70, 16448\n"
"v_fma_f32 v73, v73, v9, v17\n"
"v_mfma_f32_16x16x32_bf16 v[88:91], a[32:35], v[36:39], v[88:91]\n"
"v_fma_f32 v74, v74, v10, v18\n"
"v_mfma_f32_16x16x32_bf16 v[92:95], a[36:39], v[36:39], v[92:95]\n"
"s_cmp_eq_u32 s70, 148032\ns_cselect_b32 s70, 0, s70\n"
"v_mfma_f32_16x16x32_bf16 v[80:83], a[40:43], v[36:39], v[80:83]\n"
"v_fma_f32 v75, v75, v11, v19\n"
"v_mfma_f32_16x16x32_bf16 v[84:87], a[44:47], v[36:39], v[84:87]\n"
"s_waitcnt lgkmcnt(2)\n"
"v_mfma_f32_16x16x32_bf16 v[96:99], a[48:51], v[40:43], v[96:99]\n"
"ds_read_b128 v[36:39], v0 offset:2112\n"
"v_fma_f32 v76, v76, v12, v20\n"
"v_mfma_f32_16x16x32_bf16 v[100:103], a[52:55], v[40:43], v[100:103]\n"
"v_fma_f32 v77, v77, v13, v21\n"
"v_mfma_f32_16x16x32_bf16 v[88:91], a[56:59], v[40:43], v[88:91]\n"
"s_mov_b32 m0, s68\n"
"v_mfma_f32_16x16x32_bf16 v[92:95], a[60:63], v[40:43], v[92:95]\n"
"v_fma_f32 v78, v78, v14, v22\n"
"v_mfma_f32_16x16x32_bf16 v[80:83], a[64:67], v[40:43], v[80:83]\n"
"buffer_load_dwordx4 v2, s[36:39], s79 offen nt lds\n"
"v_mfma_f32_16x16x32_bf16 v[84:87], a[68:71], v[40:43], v[84:87]\n"
"v_fma_f32 v79, v79, v15, v23\n"
"s_waitcnt lgkmcnt(2)\n"
"v_mfma_f32_16x16x32_bf16 v[96:99], a[72:75], v[44:47], v[96:99]\n"
"ds_read_b128 v[40:43], v0 offset:3072\n"
"v_mfma_f32_16x16x32_bf16 v[100:103], a[76:79], v[44:47], v[100:103]\n"
"v_exp_f32 v24, v72\n"
"v_mfma_f32_16x16x32_bf16 v[88:91], a[80:83], v[44:47], v[88:91]\n"
"v_exp_f32 v25, v73\n"
"v_mfma_f32_16x16x32_bf16 v[92:95], a[84:87], v[44:47], v[92:95]\n"
"s_add_u32 m0, s68, 8256\n"
"v_mfma_f32_16x16x32_bf16 v[80:83], a[88:91], v[44:47], v[80:83]\n"
"v_mfma_f32_16x16x32_bf16 v[84:87], a[92:95], v[44:47], v[84:87]\n"
"s_waitcnt lgkmcnt(2)\n"
"v_mfma_f32_16x16x32_bf16 v[96:99], a[96:99], v[32:35], v[96:99]\n"
"ds_read_b128 v[44:47], v0 offset:3136\n"
"v_mfma_f32_16x16x32_bf16 v[100:103], a[100:103], v[32:35], v[100:103]\n"
"buffer_load_dwordx4 v2, s[40:43], s79 offen nt lds\n"
"v_mfma_f32_16x16x32_bf16 v[88:91], a[104:107], v[32:35], v[88:91]\n"
"v_exp_f32 v26, v74\n"
"v_mfma_f32_16x16x32_bf16 v[92:95], a[108:111], v[32:35], v[92:95]\n"
"v_exp_f32 v27, v75\n"
"v_mfma_f32_16x16x32_bf16 v[80:83], a[112:115], v[32:35], v[80:83]\n"
"s_add_u32 s79, s79, 1024\n"
"v_mfma_f32_16x16x32_bf16 v[84:87], a[116:119], v[32:35], v[84:87]\n"
"s_waitcnt lgkmcnt(2)\n"
"v_mfma_f32_16x16x32_bf16 v[96:99], a[120:123], v[36:39], v[96:99]\n"
"ds_read_b128 v[32:35], v0 offset:4096\n"
"v_mfma_f32_16x16x32_bf16 v[100:103], a[124:127], v[36:39], v[100:103]\n"
"s_add_u32 m0, s68, 1024\n"
"v_mfma_f32_16x16x32_bf16 v[88:91], a[128:131], v[36:39], v[88:91]\n"
"v_exp_f32 v28, v76\n"
"v_mfma_f32_16x16x32_bf16 v[92:95], a[132:135], v[36:39], v[92:95]\n"
"buffer_load_dwordx4 v2, s[36:39], s80 offen nt lds\n"
"v_mfma_f32_16x16x32_bf16 v[80:83], a[136:139], v[36:39], v[80:83]\n"
"v_exp_f32 v29, v77\n"
"v_mfma_f32_16x16x32_bf16 v[84:87], a[140:143], v[36:39], v[84:87]\n"
"s_waitcnt lgkmcnt(2)\n"
"v_mfma_f32_16x16x32_bf16 v[96:99], a[144:147], v[40:43], v[96:99]\n"
"ds_read_b128 v[36:39], v0 offset:4160\n"
"v_mfma_f32_16x16x32_bf16 v[100:103], a[148:151], v[40:43], v[100:103]\n"
"v_exp_f32 v30, v78\n"
"v_mfma_f32_16x16x32_bf16 v[88:91], a[152:155], v[40:43], v[88:91]\n"
"s_add_u32 m0, s68, 9280\n"
"v_mfma_f32_16x16x32_bf16 v[92:95], a[156:159], v[40:43], v[92:95]\n"
"v_exp_f32 v31, v79\n"
"v_mfma_f32_16x16x32_bf16 v[80:83], a[160:163], v[40:43], v[80:83]\n"
"buffer_load_dwordx4 v2, s[40:43], s80 offen nt lds\n"
"v_mfma_f32_16x16x32_bf16 v[84:87], a[164:167], v[40:43], v[84:87]\n"
"s_waitcnt lgkmcnt(2)\n"
"v_mfma_f32_16x16x32_bf16 v[96:99], a[168:171], v[44:47], v[96:99]\n"
"ds_read_b128 v[40:43], v0 offset:5120\n"
"v_fma_f32 v24, v24, s83, s83\n"
"v_mfma_f32_16x16x32_bf16 v[100:103], a[172:175], v[44:47], v[100:103]\n"
"v_fma_f32 v25, v25, s83, s83\n"
"v_mfma_f32_16x16x32_bf16 v[88:91], a[176:179], v[44:47], v[88:91]\n"
"v_fma_f32 v26, v26, s83, s83\n"
"v_mfma_f32_16x16x32_bf16 v[92:95], a[180:183], v[44:47], v[92:95]\n"
"s_add_u32 s80, s80, 1024\n"
"v_mfma_f32_16x16x32_bf16 v[80:83], a[184:187], v[44:47], v[80:83]\n"
"v_fma_f32 v27, v27, s83, s83\n"
"v_mfma_f32_16x16x32_bf16 v[84:87], a[188:191], v[44:47], v[84:87]\n"
"v_fma_f32 v28, v28, s83, s83\n"
"s_waitcnt lgkmcnt(2)\n"
"v_mfma_f32_16x16x32_bf16 v[96:99], a[192:195], v[32:35], v[96:99]\n"
"ds_read_b128 v[44:47], v0 offset:5184\n"
"s_add_u32 s68, s68, 16448\n"
"v_mfma_f32_16x16x32_bf16 v[100:103], a[196:199], v[32:35], v[100:103]\n"
"v_fma_f32 v29, v29, s83, s83\n"
"v_mfma_f32_16x16x32_bf16 v[88:91], a[200:203], v[32:35], v[88:91]\n"
"s_cmp_eq_u32 s68, s69\ns_cselect_b32 s68, s81, s68\n"
"v_mfma_f32_16x16x32_bf16 v[92:95], a[204:207], v[32:35], v[92:95]\n"
"v_fma_f32 v30, v30, s83, s83\n"
"v_mfma_f32_16x16x32_bf16 v[80:83], a[208:211], v[32:35], v[80:83]\n"
"v_fma_f32 v31, v31, s83, s83\n"
"v_mfma_f32_16x16x32_bf16 v[84:87], a[212:215], v[32:35], v[84:87]\n"
"s_waitcnt lgkmcnt(2)\n"
"v_mfma_f32_16x16x32_bf16 v[96:99], a[216:219], v[36:39], v[96:99]\n"
"ds_read_b128 v[32:35], v0 offset:6144\n"
"v_mfma_f32_16x16x32_bf16 v[100:103], a[220:223], v[36:39], v[100:103]\n"
"v_rcp_f32 v24, v24\n"
"v_mfma_f32_16x16x32_bf16 v[88:91], a[224:227], v[36:39], v[88:91]\n"
"v_rcp_f32 v25, v25\n"
"v_mfma_f32_16x16x32_bf16 v[92:95], a[228:231], v[36:39], v[92:95]\n"
"v_mfma_f32_16x16x32_bf16 v[80:83], a[232:235], v[36:39], v[80:83]\n"
"v_rcp_f32 v26, v26\n"
"v_mfma_f32_16x16x32_bf16 v[84:87], a[236:239], v[36:39], v[84:87]\n"
"s_waitcnt lgkmcnt(2)\n"
"v_mfma_f32_16x16x32_bf16 v[96:99], a[240:243], v[40:43], v[96:99]\n"
"ds_read_b128 v[36:39], v0 offset:6208\n"
"v_mfma_f32_16x16x32_bf16 v[100:103], a[244:247], v[40:43], v[100:103]\n"
"v_rcp_f32 v27, v27\n"
"v_mfma_f32_16x16x32_bf16 v[88:91], a[248:251], v[40:43], v[88:91]\n"
"v_rcp_f32 v28, v28\n"
"v_mfma_f32_16x16x32_bf16 v[92:95], a[252:255], v[40:43], v[92:95]\n"
"v_mfma_f32_16x16x32_bf16 v[80:83], v[128:131], v[40:43], v[80:83]\n"
"v_rcp_f32 v29, v29\n"
"v_mfma_f32_16x16x32_bf16 v[84:87], v[132:135], v[40:43], v[84:87]\n"
"s_waitcnt lgkmcnt(2)\n"
"v_mfma_f32_16x16x32_bf16 v[96:99], v[136:139], v[44:47], v[96:99]\n"
"ds_read_b128 v[40:43], v0 offset:7168\n"
"v_mfma_f32_16x16x32_bf16 v[100:103], v[140:143], v[44:47], v[100:103]\n"
"v_rcp_f32 v30, v30\n"
"v_mfma_f32_16x16x32_bf16 v[88:91], v[144:147], v[44:47], v[88:91]\n"
"v_mfma_f32_16x16x32_bf16 v[92:95], v[148:151], v[44:47], v[92:95]\n"
"v_rcp_f32 v31, v31\n"
"v_mfma_f32_16x16x32_bf16 v[80:83], v[152:155], v[44:47], v[80:83]\n"
"v_mfma_f32_16x16x32_bf16 v[84:87], v[156:159], v[44:47], v[84:87]\n"
"v_mul_f32 v72, v72, v24\n"
"s_waitcnt lgkmcnt(2)\n"
"v_mfma_f32_16x16x32_bf16 v[96:99], v[160:163], v[32:35], v[96:99]\n"
"ds_read_b128 v[44:47], v0 offset:7232\n"
"v_mfma_f32_16x16x32_bf16 v[100:103], v[164:167], v[32:35], v[100:103]\n"
"v_add_u32 v0, s70, v1\n"
"v_mul_f32 v73, v73, v25\n"
"v_mfma_f32_16x16x32_bf16 v[88:91], v[168:171], v[32:35], v[88:91]\n"
"v_mul_f32 v74, v74, v26\n"
"v_mfma_f32_16x16x32_bf16 v[92:95], v[172:175], v[32:35], v[92:95]\n"
"v_mul_f32 v75, v75, v27\n"
"v_mfma_f32_16x16x32_bf16 v[80:83], v[176:179], v[32:35], v[80:83]\n"
"v_mul_f32 v76, v76, v28\n"
"v_mfma_f32_16x16x32_bf16 v[84:87], v[180:183], v[32:35], v[84:87]\n"
"s_waitcnt lgkmcnt(2)\n"
"v_mfma_f32_16x16x32_bf16 v[96:99], v[184:187], v[36:39], v[96:99]\n"
"ds_read_b128 v[32:35], v0 offset:0\n"
"v_mul_f32 v77, v77, v29\n"
"v_mfma_f32_16x16x32_bf16 v[100:103], v[188:191], v[36:39], v[100:103]\n"
"v_mul_f32 v78, v78, v30\n"
"v_mfma_f32_16x16x32_bf16 v[88:91], v[192:195], v[36:39], v[88:91]\n"
"v_mul_f32 v79, v79, v31\n"
"v_mfma_f32_16x16x32_bf16 v[92:95], v[196:199], v[36:39], v[92:95]\n"
"v_cvt_pk_bf16_f32 v72, v72, v73\n"
"v_mfma_f32_16x16x32_bf16 v[80:83], v[200:203], v[36:39], v[80:83]\n"
"v_cvt_pk_bf16_f32 v73, v74, v75\n"
"v_mfma_f32_16x16x32_bf16 v[84:87], v[204:207], v[36:39], v[84:87]\n"
"v_cvt_pk_bf16_f32 v74, v76, v77\n"
"s_waitcnt lgkmcnt(2)\n"
"v_mfma_f32_16x16x32_bf16 v[96:99], v[208:211], v[40:43], v[96:99]\n"
"ds_read_b128 v[36:39], v0 offset:64\n"
"v_mfma_f32_16x16x32_bf16 v[100:103], v[212:215], v[40:43], v[100:103]\n"
"v_cvt_pk_bf16_f32 v75, v78, v79\n"
"v_mfma_f32_16x16x32_bf16 v[88:91], v[216:219], v[40:43], v[88:91]\n"
"buffer_store_dwordx4 v[72:75], v3, s[56:59], s82 offen offset:1024 sc1 nt\n"
"v_mfma_f32_16x16x32_bf16 v[92:95], v[220:223], v[40:43], v[92:95]\n"
"v_mfma_f32_16x16x32_bf16 v[80:83], v[224:227], v[40:43], v[80:83]\n"
"v_mfma_f32_16x16x32_bf16 v[84:87], v[228:231], v[40:43], v[84:87]\n"
"s_waitcnt lgkmcnt(2)\n"
"v_mfma_f32_16x16x32_bf16 v[96:99], v[232:235], v[44:47], v[96:99]\n"
"ds_read_b128 v[40:43], v0 offset:1024\n"
"v_mfma_f32_16x16x32_bf16 v[100:103], v[236:239], v[44:47], v[100:103]\n"
"v_mfma_f32_16x16x32_bf16 v[88:91], v[240:243], v[44:47], v[88:91]\n"
"v_mfma_f32_16x16x32_bf16 v[92:95], v[244:247], v[44:47], v[92:95]\n"
"v_mfma_f32_16x16x32_bf16 v[80:83], v[248:251], v[44:47], v[80:83]\n"
"v_mfma_f32_16x16x32_bf16 v[84:87], v[252:255], v[44:47], v[84:87]\n"
"s_waitcnt lgkmcnt(2)\n"
"v_mfma_f32_16x16x32_bf16 v[72:75], a[0:3], v[32:35], 0\n"
"ds_read_b128 v[44:47], v0 offset:1088\n"
"v_mfma_f32_16x16x32_bf16 v[76:79], a[4:7], v[32:35], 0\n"
"s_add_u32 s70, s70, 16448\n"
"v_mfma_f32_16x16x32_bf16 v[96:99], a[8:11], v[32:35], v[96:99]\n"
"v_mfma_f32_16x16x32_bf16 v[100:103], a[12:15], v[32:35], v[100:103]\n"
"s_cmp_eq_u32 s70, 148032\ns_cselect_b32 s70, 0, s70\n"
"v_mfma_f32_16x16x32_bf16 v[88:91], a[16:19], v[32:35], v[88:91]\n"
"v_mfma_f32_16x16x32_bf16 v[92:95], a[20:23], v[32:35], v[92:95]\n"
"s_waitcnt lgkmcnt(2)\n"
"v_mfma_f32_16x16x32_bf16 v[72:75], a[24:27], v[36:39], v[72:75]\n"
"ds_read_b128 v[32:35], v0 offset:2048\n"
"v_fma_f32 v80, v80, v8, v16\n"
"v_mfma_f32_16x16x32_bf16 v[76:79], a[28:31], v[36:39], v[76:79]\n"
"v_fma_f32 v81, v81, v9, v17\n"
"v_mfma_f32_16x16x32_bf16 v[96:99], a[32:35], v[36:39], v[96:99]\n"
"s_mov_b32 m0, s68\n"
"v_fma_f32 v82, v82, v10, v18\n"
"v_mfma_f32_16x16x32_bf16 v[100:103], a[36:39], v[36:39], v[100:103]\n"
"v_fma_f32 v83, v83, v11, v19\n"
"v_mfma_f32_16x16x32_bf16 v[88:91], a[40:43], v[36:39], v[88:91]\n"
"v_fma_f32 v84, v84, v12, v20\n"
"v_mfma_f32_16x16x32_bf16 v[92:95], a[44:47], v[36:39], v[92:95]\n"
"s_waitcnt lgkmcnt(2)\n"
"v_mfma_f32_16x16x32_bf16 v[72:75], a[48:51], v[40:43], v[72:75]\n"
"ds_read_b128 v[36:39], v0 offset:2112\n"
"v_fma_f32 v85, v85, v13, v21\n"
"v_mfma_f32_16x16x32_bf16 v[76:79], a[52:55], v[40:43], v[76:79]\n"
"buffer_load_dwordx4 v2, s[36:39], s79 offen nt lds\n"
"v_mfma_f32_16x16x32_bf16 v[96:99], a[56:59], v[40:43], v[96:99]\n"
"v_fma_f32 v86, v86, v14, v22\n"
"v_mfma_f32_16x16x32_bf16 v[100:103], a[60:63], v[40:43], v[100:103]\n"
"v_fma_f32 v87, v87, v15, v23\n"
"v_mfma_f32_16x16x32_bf16 v[88:91], a[64:67], v[40:43], v[88:91]\n"
"s_add_u32 m0, s68, 8256\n"
"v_mfma_f32_16x16x32_bf16 v[92:95], a[68:71], v[40:43], v[92:95]\n"
"s_waitcnt lgkmcnt(2)\n"
"v_mfma_f32_16x16x32_bf16 v[72:75], a[72:75], v[44:47], v[72:75]\n"
"ds_read_b128 v[40:43], v0 offset:3072\n"
"v_mfma_f32_16x16x32_bf16 v[76:79], a[76:79], v[44:47], v[76:79]\n"
"buffer_load_dwordx4 v2, s[40:43], s79 offen nt lds\n"
"v_mfma_f32_16x16x32_bf16 v[96:99], a[80:83], v[44:47], v[96:99]\n"
"v_exp_f32 v24, v80\n"
"v_mfma_f32_16x16x32_bf16 v[100:103], a[84:87], v[44:47], v[100:103]\n"
"v_exp_f32 v25, v81\n"
"v_mfma_f32_16x16x32_bf16 v[88:91], a[88:91], v[44:47], v[88:91]\n"
"v_exp_f32 v26, v82\n"
"v_mfma_f32_16x16x32_bf16 v[92:95], a[92:95], v[44:47], v[92:95]\n"
"s_waitcnt lgkmcnt(2)\n"
"v_mfma_f32_16x16x32_bf16 v[72:75], a[96:99], v[32:35], v[72:75]\n"
"ds_read_b128 v[44:47], v0 offset:3136\n"
"v_mfma_f32_16x16x32_bf16 v[76:79], a[100:103], v[32:35], v[76:79]\n"
"s_add_u32 s79, s79, 1024\n"
"v_mfma_f32_16x16x32_bf16 v[96:99], a[104:107], v[32:35], v[96:99]\n"
"v_exp_f32 v27, v83\n"
"v_mfma_f32_16x16x32_bf16 v[100:103], a[108:111], v[32:35], v[100:103]\n"
"s_add_u32 m0, s68, 1024\n"
"v_mfma_f32_16x16x32_bf16 v[88:91], a[112:115], v[32:35], v[88:91]\n"
"v_exp_f32 v28, v84\n"
"v_mfma_f32_16x16x32_bf16 v[92:95], a[116:119], v[32:35], v[92:95]\n"
"s_waitcnt lgkmcnt(2)\n"
"v_mfma_f32_16x16x32_bf16 v[72:75], a[120:123], v[36:39], v[72:75]\n"
"ds_read_b128 v[32:35], v0 offset:4096\n"
"v_mfma_f32_16x16x32_bf16 v[76:79], a[124:127], v[36:39], v[76:79]\n"
"buffer_load_dwordx4 v2, s[36:39], s80 offen nt lds\n"
"v_mfma_f32_16x16x32_bf16 v[96:99], a[128:131], v[36:39], v[96:99]\n"
"v_exp_f32 v29, v85\n"
"v_mfma_f32_16x16x32_bf16 v[100:103], a[132:135], v[36:39], v[100:103]\n"
"v_mfma_f32_16x16x32_bf16 v[88:91], a[136:139], v[36:39], v[88:91]\n"
"v_exp_f32 v30, v86\n"
"v_mfma_f32_16x16x32_bf16 v[92:95], a[140:143], v[36:39], v[92:95]\n"
"s_waitcnt lgkmcnt(2)\n"
"v_mfma_f32_16x16x32_bf16 v[72:75], a[144:147], v[40:43], v[72:75]\n"
"ds_read_b128 v[36:39], v0 offset:4160\n"
"s_add_u32 m0, s68, 9280\n"
"v_mfma_f32_16x16x32_bf16 v[76:79], a[148:151], v[40:43], v[76:79]\n"
"v_exp_f32 v31, v87\n"
"v_mfma_f32_16x16x32_bf16 v[96:99], a[152:155], v[40:43], v[96:99]\n"
"buffer_load_dwordx4 v2, s[40:43], s80 offen nt lds\n"
"v_mfma_f32_16x16x32_bf16 v[100:103], a[156:159], v[40:43], v[100:103]\n"
"v_mfma_f32_16x16x32_bf16 v[88:91], a[160:163], v[40:43], v[88:91]\n"
"v_fma_f32 v24, v24, s83, s83\n"
"v_mfma_f32_16x16x32_bf16 v[92:95], a[164:167], v[40:43], v[92:95]\n"
"s_waitcnt lgkmcnt(2)\n"
"v_mfma_f32_16x16x32_bf16 v[72:75], a[168:171], v[44:47], v[72:75]\n"
"ds_read_b128 v[40:43], v0 offset:5120\n"
"v_fma_f32 v25, v25, s83, s83\n"
"v_mfma_f32_16x16x32_bf16 v[76:79], a[172:175], v[44:47], v[76:79]\n"
"v_fma_f32 v26, v26, s83, s83\n"
"v_mfma_f32_16x16x32_bf16 v[96:99], a[176:179], v[44:47], v[96:99]\n"
"s_add_u32 s80, s80, 1024\n"
"v_mfma_f32_16x16x32_bf16 v[100:103], a[180:183], v[44:47], v[100:103]\n"
"v_fma_f32 v27, v27, s83, s83\n"
"v_mfma_f32_16x16x32_bf16 v[88:91], a[184:187], v[44:47], v[88:91]\n"
"s_add_u32 s68, s68, 16448\n"
"v_fma_f32 v28, v28, s83, s83\n"
"v_mfma_f32_16x16x32_bf16 v[92:95], a[188:191], v[44:47], v[92:95]\n"
"s_waitcnt lgkmcnt(2)\n"
"v_mfma_f32_16x16x32_bf16 v[72:75], a[192:195], v[32:35], v[72:75]\n"
"ds_read_b128 v[44:47], v0 offset:5184\n"
"v_fma_f32 v29, v29, s83, s83\n"
"v_mfma_f32_16x16x32_bf16 v[76:79], a[196:199], v[32:35], v[76:79]\n"
"s_cmp_eq_u32 s68, s69\ns_cselect_b32 s68, s81, s68\n"
"v_mfma_f32_16x16x32_bf16 v[96:99], a[200:203], v[32:35], v[96:99]\n"
"v_fma_f32 v30, v30, s83, s83\n"
"v_mfma_f32_16x16x32_bf16 v[100:103], a[204:207], v[32:35], v[100:103]\n"
"v_fma_f32 v31, v31, s83, s83\n"
"v_mfma_f32_16x16x32_bf16 v[88:91], a[208:211], v[32:35], v[88:91]\n"
"v_rcp_f32 v24, v24\n"
"v_mfma_f32_16x16x32_bf16 v[92:95], a[212:215], v[32:35], v[92:95]\n"
"s_waitcnt lgkmcnt(2)\n"
"v_mfma_f32_16x16x32_bf16 v[72:75], a[216:219], v[36:39], v[72:75]\n"
"ds_read_b128 v[32:35], v0 offset:6144\n"
"v_mfma_f32_16x16x32_bf16 v[76:79], a[220:223], v[36:39], v[76:79]\n"
"v_rcp_f32 v25, v25\n"
"v_mfma_f32_16x16x32_bf16 v[96:99], a[224:227], v[36:39], v[96:99]\n"
"v_mfma_f32_16x16x32_bf16 v[100:103], a[228:231], v[36:39], v[100:103]\n"
"v_rcp_f32 v26, v26\n"
"v_mfma_f32_16x16x32_bf16 v[88:91], a[232:235], v[36:39], v[88:91]\n"
"v_mfma_f32_16x16x32_bf16 v[92:95], a[236:239], v[36:39], v[92:95]\n"
"s_waitcnt lgkmcnt(2)\n"
"v_mfma_f32_16x16x32_bf16 v[72:75], a[240:243], v[40:43], v[72:75]\n"
"ds_read_b128 v[36:39], v0 offset:6208\n"
"v_mfma_f32_16x16x32_bf16 v[76:79], a[244:247], v[40:43], v[76:79]\n"
"v_rcp_f32 v27, v27\n"
"v_mfma_f32_16x16x32_bf16 v[96:99], a[248:251], v[40:43], v[96:99]\n"
"v_rcp_f32 v28, v28\n"
"v_mfma_f32_16x16x32_bf16 v[100:103], a[252:255], v[40:43], v[100:103]\n"
"v_rcp_f32 v29, v29\n"
"v_mfma_f32_16x16x32_bf16 v[88:91], v[128:131], v[40:43], v[88:91]\n"
"v_mfma_f32_16x16x32_bf16 v[92:95], v[132:135], v[40:43], v[92:95]\n"
"s_waitcnt lgkmcnt(2)\n"
"v_mfma_f32_16x16x32_bf16 v[72:75], v[136:139], v[44:47], v[72:75]\n"
"ds_read_b128 v[40:43], v0 offset:7168\n"
"v_mfma_f32_16x16x32_bf16 v[76:79], v[140:143], v[44:47], v[76:79]\n"
"v_rcp_f32 v30, v30\n"
"v_mfma_f32_16x16x32_bf16 v[96:99], v[144:147], v[44:47], v[96:99]\n"
"v_rcp_f32 v31, v31\n"
"v_mfma_f32_16x16x32_bf16 v[100:103], v[148:151], v[44:47], v[100:103]\n"
"v_mfma_f32_16x16x32_bf16 v[88:91], v[152:155], v[44:47], v[88:91]\n"
"v_mul_f32 v80, v80, v24\n"
"v_mfma_f32_16x16x32_bf16 v[92:95], v[156:159], v[44:47], v[92:95]\n"
"s_waitcnt vmcnt(12)\n"
"s_barrier\n"
"s_waitcnt lgkmcnt(2)\n"
"v_mfma_f32_16x16x32_bf16 v[72:75], v[160:163], v[32:35], v[72:75]\n"
"ds_read_b128 v[44:47], v0 offset:7232\n"
"v_mfma_f32_16x16x32_bf16 v[76:79], v[164:167], v[32:35], v[76:79]\n"
"v_add_u32 v0, s70, v1\n"
"v_mul_f32 v81, v81, v25\n"
"v_mfma_f32_16x16x32_bf16 v[96:99], v[168:171], v[32:35], v[96:99]\n"
"v_mul_f32 v82, v82, v26\n"
"v_mfma_f32_16x16x32_bf16 v[100:103], v[172:175], v[32:35], v[100:103]\n"
"v_mul_f32 v83, v83, v27\n"
"v_mfma_f32_16x16x32_bf16 v[88:91], v[176:179], v[32:35], v[88:91]\n"
"v_mul_f32 v84, v84, v28\n"
"v_mfma_f32_16x16x32_bf16 v[92:95], v[180:183], v[32:35], v[92:95]\n"
"v_mul_f32 v85, v85, v29\n"
"s_waitcnt lgkmcnt(2)\n"
"v_mfma_f32_16x16x32_bf16 v[72:75], v[184:187], v[36:39], v[72:75]\n"
"ds_read_b128 v[32:35], v0 offset:0\n"
"v_mfma_f32_16x16x32_bf16 v[76:79], v[188:191], v[36:39], v[76:79]\n"
"v_mul_f32 v86, v86, v30\n"
"v_mfma_f32_16x16x32_bf16 v[96:99], v[192:195], v[36:39], v[96:99]\n"
"v_mul_f32 v87, v87, v31\n"
"v_mfma_f32_16x16x32_bf16 v[100:103], v[196:199], v[36:39], v[100:103]\n"
"v_cvt_pk_bf16_f32 v80, v80, v81\n"
"v_mfma_f32_16x16x32_bf16 v[88:91], v[200:203], v[36:39], v[88:91]\n"
"v_cvt_pk_bf16_f32 v81, v82, v83\n"
"v_mfma_f32_16x16x32_bf16 v[92:95], v[204:207], v[36:39], v[92:95]\n"
"v_cvt_pk_bf16_f32 v82, v84, v85\n"
"s_waitcnt lgkmcnt(2)\n"
"v_mfma_f32_16x16x32_bf16 v[72:75], v[208:211], v[40:43], v[72:75]\n"
"ds_read_b128 v[36:39], v0 offset:64\n"
"v_mfma_f32_16x16x32_bf16 v[76:79], v[212:215], v[40:43], v[76:79]\n"
"v_cvt_pk_bf16_f32 v83, v86, v87\n"
"v_mfma_f32_16x16x32_bf16 v[96:99], v[216:219], v[40:43], v[96:99]\n"
"buffer_store_dwordx4 v[80:83], v3, s[56:59], s82 offen offset:2048 sc1 nt\n"
"v_mfma_f32_16x16x32_bf16 v[100:103], v[220:223], v[40:43], v[100:103]\n"
"v_mfma_f32_16x16x32_bf16 v[88:91], v[224:227], v[40:43], v[88:91]\n"
"v_mfma_f32_16x16x32_bf16 v[92:95], v[228:231], v[40:43], v[92:95]\n"
"s_waitcnt lgkmcnt(2)\n"
"v_mfma_f32_16x16x32_bf16 v[72:75], v[232:235], v[44:47], v[72:75]\n"
"ds_read_b128 v[40:43], v0 offset:1024\n"
"v_mfma_f32_16x16x32_bf16 v[76:79], v[236:239], v[44:47], v[76:79]\n"
"v_mfma_f32_16x16x32_bf16 v[96:99], v[240:243], v[44:47], v[96:99]\n"
"v_mfma_f32_16x16x32_bf16 v[100:103], v[244:247], v[44:47], v[100:103]\n"
"v_mfma_f32_16x16x32_bf16 v[88:91], v[248:251], v[44:47], v[88:91]\n"
"v_mfma_f32_16x16x32_bf16 v[92:95], v[252:255], v[44:47], v[92:95]\n"
"s_waitcnt lgkmcnt(2)\n"
"v_mfma_f32_16x16x32_bf16 v[80:83], a[0:3], v[32:35], 0\n"
"ds_read_b128 v[44:47], v0 offset:1088\n"
"v_mfma_f32_16x16x32_bf16 v[84:87], a[4:7], v[32:35], 0\n"
"s_add_u32 s70, s70, 16448\n"
"v_mfma_f32_16x16x32_bf16 v[72:75], a[8:11], v[32:35], v[72:75]\n"
"v_mfma_f32_16x16x32_bf16 v[76:79], a[12:15], v[32:35], v[76:79]\n"
"s_cmp_eq_u32 s70, 148032\ns_cselect_b32 s70, 0, s70\n"
"v_mfma_f32_16x16x32_bf16 v[96:99], a[16:19], v[32:35], v[96:99]\n"
"v_mfma_f32_16x16x32_bf16 v[100:103], a[20:23], v[32:35], v[100:103]\n"
"s_waitcnt lgkmcnt(2)\n"
"v_mfma_f32_16x16x32_bf16 v[80:83], a[24:27], v[36:39], v[80:83]\n"
"ds_read_b128 v[32:35], v0 offset:2048\n"
"v_fma_f32 v88, v88, v8, v16\n"
"v_mfma_f32_16x16x32_bf16 v[84:87], a[28:31], v[36:39], v[84:87]\n"
"v_fma_f32 v89, v89, v9, v17\n"
"v_mfma_f32_16x16x32_bf16 v[72:75], a[32:35], v[36:39], v[72:75]\n"
"s_mov_b32 m0, s68\n"
"v_fma_f32 v90, v90, v10, v18\n"
"v_mfma_f32_16x16x32_bf16 v[76:79], a[36:39], v[36:39], v[76:79]\n"
"v_fma_f32 v91, v91, v11, v19\n"
"v_mfma_f32_16x16x32_bf16 v[96:99], a[40:43], v[36:39], v[96:99]\n"
"v_fma_f32 v92, v92, v12, v20\n"
"v_mfma_f32_16x16x32_bf16 v[100:103], a[44:47], v[36:39], v[100:103]\n"
"s_waitcnt lgkmcnt(2)\n"
"v_mfma_f32_16x16x32_bf16 v[80:83], a[48:51], v[40:43], v[80:83]\n"
"ds_read_b128 v[36:39], v0 offset:2112\n"
"v_fma_f32 v93, v93, v13, v21\n"
"v_mfma_f32_16x16x32_bf16 v[84:87], a[52:55], v[40:43], v[84:87]\n"
"buffer_load_dwordx4 v2, s[36:39], s79 offen nt lds\n"
"v_mfma_f32_16x16x32_bf16 v[72:75], a[56:59], v[40:43], v[72:75]\n"
"v_fma_f32 v94, v94, v14, v22\n"
"v_mfma_f32_16x16x32_bf16 v[76:79], a[60:63], v[40:43], v[76:79]\n"
"v_fma_f32 v95, v95, v15, v23\n"
"v_mfma_f32_16x16x32_bf16 v[96:99], a[64:67], v[40:43], v[96:99]\n"
"s_add_u32 m0, s68, 8256\n"
"v_mfma_f32_16x16x32_bf16 v[100:103], a[68:71], v[40:43], v[100:103]\n"
"s_waitcnt lgkmcnt(2)\n"
"v_mfma_f32_16x16x32_bf16 v[80:83], a[72:75], v[44:47], v[80:83]\n"
"ds_read_b128 v[40:43], v0 offset:3072\n"
"v_mfma_f32_16x16x32_bf16 v[84:87], a[76:79], v[44:47], v[84:87]\n"
"buffer_load_dwordx4 v2, s[40:43], s79 offen nt lds\n"
"v_mfma_f32_16x16x32_bf16 v[72:75], a[80:83], v[44:47], v[72:75]\n"
"v_exp_f32 v24, v88\n"
"v_mfma_f32_16x16x32_bf16 v[76:79], a[84:87], v[44:47], v[76:79]\n"
"v_exp_f32 v25, v89\n"
"v_mfma_f32_16x16x32_bf16 v[96:99], a[88:91], v[44:47], v[96:99]\n"
"v_exp_f32 v26, v90\n"
"v_mfma_f32_16x16x32_bf16 v[100:103], a[92:95], v[44:47], v[100:103]\n"
"s_waitcnt lgkmcnt(2)\n"
"v_mfma_f32_16x16x32_bf16 v[80:83], a[96:99], v[32:35], v[80:83]\n"
"ds_read_b128 v[44:47], v0 offset:3136\n"
"v_mfma_f32_16x16x32_bf16 v[84:87], a[100:103], v[32:35], v[84:87]\n"
"s_add_u32 s79, s79, 1024\n"
"v_mfma_f32_16x16x32_bf16 v[72:75], a[104:107], v[32:35], v[72:75]\n"
"v_exp_f32 v27, v91\n"
"v_mfma_f32_16x16x32_bf16 v[76:79], a[108:111], v[32:35], v[76:79]\n"
"s_add_u32 m0, s68, 1024\n"
"v_mfma_f32_16x16x32_bf16 v[96:99], a[112:115], v[32:35], v[96:99]\n"
"v_exp_f32 v28, v92\n"
"v_mfma_f32_16x16x32_bf16 v[100:103], a[116:119], v[32:35], v[100:103]\n"
"s_waitcnt lgkmcnt(2)\n"
"v_mfma_f32_16x16x32_bf16 v[80:83], a[120:123], v[36:39], v[80:83]\n"
"ds_read_b128 v[32:35], v0 offset:4096\n"
"v_mfma_f32_16x16x32_bf16 v[84:87], a[124:127], v[36:39], v[84:87]\n"
"buffer_load_dwordx4 v2, s[36:39], s80 offen nt lds\n"
"v_mfma_f32_16x16x32_bf16 v[72:75], a[128:131], v[36:39], v[72:75]\n"
"v_exp_f32 v29, v93\n"
"v_mfma_f32_16x16x32_bf16 v[76:79], a[132:135], v[36:39], v[76:79]\n"
"v_mfma_f32_16x16x32_bf16 v[96:99], a[136:139], v[36:39], v[96:99]\n"
"v_exp_f32 v30, v94\n"
"v_mfma_f32_16x16x32_bf16 v[100:103], a[140:143], v[36:39], v[100:103]\n"
"s_waitcnt lgkmcnt(2)\n"
"v_mfma_f32_16x16x32_bf16 v[80:83], a[144:147], v[40:43], v[80:83]\n"
"ds_read_b128 v[36:39], v0 offset:4160\n"
"s_add_u32 m0, s68, 9280\n"
"v_mfma_f32_16x16x32_bf16 v[84:87], a[148:151], v[40:43], v[84:87]\n"
"v_exp_f32 v31, v95\n"
"v_mfma_f32_16x16x32_bf16 v[72:75], a[152:155], v[40:43], v[72:75]\n"
"buffer_load_dwordx4 v2, s[40:43], s80 offen nt lds\n"
"v_mfma_f32_16x16x32_bf16 v[76:79], a[156:159], v[40:43], v[76:79]\n"
"v_mfma_f32_16x16x32_bf16 v[96:99], a[160:163], v[40:43], v[96:99]\n"
"v_fma_f32 v24, v24, s83, s83\n"
"v_mfma_f32_16x16x32_bf16 v[100:103], a[164:167], v[40:43], v[100:103]\n"
"s_waitcnt lgkmcnt(2)\n"
"v_mfma_f32_16x16x32_bf16 v[80:83], a[168:171], v[44:47], v[80:83]\n"
"ds_read_b128 v[40:43], v0 offset:5120\n"
"v_fma_f32 v25, v25, s83, s83\n"
"v_mfma_f32_16x16x32_bf16 v[84:87], a[172:175], v[44:47], v[84:87]\n"
"v_fma_f32 v26, v26, s83, s83\n"
"v_mfma_f32_16x16x32_bf16 v[72:75], a[176:179], v[44:47], v[72:75]\n"
"s_add_u32 s80, s80, 1024\n"
"v_fma_f32 v27, v27, s83, s83\n"
"v_mfma_f32_16x16x32_bf16 v[76:79], a[180:183], v[44:47], v[76:79]\n"
"v_fma_f32 v28, v28, s83, s83\n"
"v_mfma_f32_16x16x32_bf16 v[96:99], a[184:187], v[44:47], v[96:99]\n"
"s_add_u32 s68, s68, 16448\n"
"v_mfma_f32_16x16x32_bf16 v[100:103], a[188:191], v[44:47], v[100:103]\n"
"v_fma_f32 v29, v29, s83, s83\n"
"s_waitcnt lgkmcnt(2)\n"
"v_mfma_f32_16x16x32_bf16 v[80:83], a[192:195], v[32:35], v[80:83]\n"
"ds_read_b128 v[44:47], v0 offset:5184\n"
"v_mfma_f32_16x16x32_bf16 v[84:87], a[196:199], v[32:35], v[84:87]\n"
"s_cmp_eq_u32 s68, s69\ns_cselect_b32 s68, s81, s68\n"
"v_mfma_f32_16x16x32_bf16 v[72:75], a[200:203], v[32:35], v[72:75]\n"
"v_fma_f32 v30, v30, s83, s83\n"
"v_mfma_f32_16x16x32_bf16 v[76:79], a[204:207], v[32:35], v[76:79]\n"
"v_fma_f32 v31, v31, s83, s83\n"
"v_mfma_f32_16x16x32_bf16 v[96:99], a[208:211], v[32:35], v[96:99]\n"
"v_rcp_f32 v24, v24\n"
"v_mfma_f32_16x16x32_bf16 v[100:103], a[212:215], v[32:35], v[100:103]\n"
"s_waitcnt lgkmcnt(2)\n"
"v_mfma_f32_16x16x32_bf16 v[80:83], a[216:219], v[36:39], v[80:83]\n"
"ds_read_b128 v[32:35], v0 offset:6144\n"
"v_mfma_f32_16x16x32_bf16 v[84:87], a[220:223], v[36:39], v[84:87]\n"
"v_rcp_f32 v25, v25\n"
"v_mfma_f32_16x16x32_bf16 v[72:75], a[224:227], v[36:39], v[72:75]\n"
"v_rcp_f32 v26, v26\n"
"v_mfma_f32_16x16x32_bf16 v[76:79], a[228:231], v[36:39], v[76:79]\n"
"v_mfma_f32_16x16x32_bf16 v[96:99], a[232:235], v[36:39], v[96:99]\n"
"v_rcp_f32 v27, v27\n"
"v_mfma_f32_16x16x32_bf16 v[100:103], a[236:239], v[36:39], v[100:103]\n"
"s_waitcnt lgkmcnt(2)\n"
"v_mfma_f32_16x16x32_bf16 v[80:83], a[240:243], v[40:43], v[80:83]\n"
"ds_read_b128 v[36:39], v0 offset:6208\n"
"v_mfma_f32_16x16x32_bf16 v[84:87], a[244:247], v[40:43], v[84:87]\n"
"v_rcp_f32 v28, v28\n"
"v_mfma_f32_16x16x32_bf16 v[72:75], a[248:251], v[40:43], v[72:75]\n"
"v_mfma_f32_16x16x32_bf16 v[76:79], a[252:255], v[40:43], v[76:79]\n"
"v_rcp_f32 v29, v29\n"
"v_mfma_f32_16x16x32_bf16 v[96:99], v[128:131], v[40:43], v[96:99]\n"
"v_mfma_f32_16x16x32_bf16 v[100:103], v[132:135], v[40:43], v[100:103]\n"
"s_waitcnt lgkmcnt(2)\n"
"v_mfma_f32_16x16x32_bf16 v[80:83], v[136:139], v[44:47], v[80:83]\n"
"ds_read_b128 v[40:43], v0 offset:7168\n"
"v_mfma_f32_16x16x32_bf16 v[84:87], v[140:143], v[44:47], v[84:87]\n"
"v_rcp_f32 v30, v30\n"
"v_mfma_f32_16x16x32_bf16 v[72:75], v[144:147], v[44:47], v[72:75]\n"
"v_rcp_f32 v31, v31\n"
"v_mfma_f32_16x16x32_bf16 v[76:79], v[148:151], v[44:47], v[76:79]\n"
"v_mfma_f32_16x16x32_bf16 v[96:99], v[152:155], v[44:47], v[96:99]\n"
"v_mul_f32 v88, v88, v24\n"
"v_mfma_f32_16x16x32_bf16 v[100:103], v[156:159], v[44:47], v[100:103]\n"
"v_mul_f32 v89, v89, v25\n"
"s_waitcnt lgkmcnt(2)\n"
"v_mfma_f32_16x16x32_bf16 v[80:83], v[160:163], v[32:35], v[80:83]\n"
"ds_read_b128 v[44:47], v0 offset:7232\n"
"v_mfma_f32_16x16x32_bf16 v[84:87], v[164:167], v[32:35], v[84:87]\n"
"v_add_u32 v0, s70, v1\n"
"v_mul_f32 v90, v90, v26\n"
"v_mfma_f32_16x16x32_bf16 v[72:75], v[168:171], v[32:35], v[72:75]\n"
"v_mul_f32 v91, v91, v27\n"
"v_mfma_f32_16x16x32_bf16 v[76:79], v[172:175], v[32:35], v[76:79]\n"
"v_mul_f32 v92, v92, v28\n"
"v_mfma_f32_16x16x32_bf16 v[96:99], v[176:179], v[32:35], v[96:99]\n"
"v_mul_f32 v93, v93, v29\n"
"v_mfma_f32_16x16x32_bf16 v[100:103], v[180:183], v[32:35], v[100:103]\n"
"v_mul_f32 v94, v94, v30\n"
"s_waitcnt lgkmcnt(2)\n"
"v_mfma_f32_16x16x32_bf16 v[80:83], v[184:187], v[36:39], v[80:83]\n"
"ds_read_b128 v[32:35], v0 offset:0\n"
"v_mfma_f32_16x16x32_bf16 v[84:87], v[188:191], v[36:39], v[84:87]\n"
"v_mul_f32 v95, v95, v31\n"
"v_mfma_f32_16x16x32_bf16 v[72:75], v[192:195], v[36:39], v[72:75]\n"
"v_cvt_pk_bf16_f32 v88, v88, v89\n"
"v_mfma_f32_16x16x32_bf16 v[76:79], v[196:199], v[36:39], v[76:79]\n"
"v_cvt_pk_bf16_f32 v89, v90, v91\n"
"v_mfma_f32_16x16x32_bf16 v[96:99], v[200:203], v[36:39], v[96:99]\n"
"v_cvt_pk_bf16_f32 v90, v92, v93\n"
"v_mfma_f32_16x16x32_bf16 v[100:103], v[204:207], v[36:39], v[100:103]\n"
"v_cvt_pk_bf16_f32 v91, v94, v95\n"
"s_waitcnt lgkmcnt(2)\n"
"v_mfma_f32_16x16x32_bf16 v[80:83], v[208:211], v[40:43], v[80:83]\n"
"ds_read_b128 v[36:39], v0 offset:64\n"
"v_mfma_f32_16x16x32_bf16 v[84:87], v[212:215], v[40:43], v[84:87]\n"
"buffer_store_dwordx4 v[88:91], v3, s[56:59], s82 offen offset:3072 sc1 nt\n"
"v_mfma_f32_16x16x32_bf16 v[72:75], v[216:219], v[40:43], v[72:75]\n"
"s_add_u32 s82, s82, 4096\n"
"v_mfma_f32_16x16x32_bf16 v[76:79], v[220:223], v[40:43], v[76:79]\n"
"v_mfma_f32_16x16x32_bf16 v[96:99], v[224:227], v[40:43], v[96:99]\n"
"v_mfma_f32_16x16x32_bf16 v[100:103], v[228:231], v[40:43], v[100:103]\n"
"s_waitcnt lgkmcnt(2)\n"
"v_mfma_f32_16x16x32_bf16 v[80:83], v[232:235], v[44:47], v[80:83]\n"
"ds_read_b128 v[40:43], v0 offset:1024\n"
"v_mfma_f32_16x16x32_bf16 v[84:87], v[236:239], v[44:47], v[84:87]\n"
"v_mfma_f32_16x16x32_bf16 v[72:75], v[240:243], v[44:47], v[72:75]\n"
"v_mfma_f32_16x16x32_bf16 v[76:79], v[244:247], v[44:47], v[76:79]\n"
"v_mfma_f32_16x16x32_bf16 v[96:99], v[248:251], v[44:47], v[96:99]\n"
"v_mfma_f32_16x16x32_bf16 v[100:103], v[252:255], v[44:47], v[100:103]\n"
"s_waitcnt lgkmcnt(2)\n"
"v_mfma_f32_16x16x32_bf16 v[88:91], a[0:3], v[32:35], 0\n"
"ds_read_b128 v[44:47], v0 offset:1088\n"
"v_mfma_f32_16x16x32_bf16 v[92:95], a[4:7], v[32:35], 0\n"
"s_add_u32 s70, s70, 16448\n"
"v_mfma_f32_16x16x32_bf16 v[80:83], a[8:11], v[32:35], v[80:83]\n"
"v_mfma_f32_16x16x32_bf16 v[84:87], a[12:15], v[32:35], v[84:87]\n"
"s_cmp_eq_u32 s70, 148032\ns_cselect_b32 s70, 0, s70\n"
"v_mfma_f32_16x16x32_bf16 v[72:75], a[16:19], v[32:35], v[72:75]\n"
"v_mfma_f32_16x16x32_bf16 v[76:79], a[20:23], v[32:35], v[76:79]\n"
"s_waitcnt lgkmcnt(2)\n"
"v_mfma_f32_16x16x32_bf16 v[88:91], a[24:27], v[36:39], v[88:91]\n"
"ds_read_b128 v[32:35], v0 offset:2048\n"
"v_fma_f32 v96, v96, v8, v16\n"
"v_mfma_f32_16x16x32_bf16 v[92:95], a[28:31], v[36:39], v[92:95]\n"
"v_fma_f32 v97, v97, v9, v17\n"
"v_mfma_f32_16x16x32_bf16 v[80:83], a[32:35], v[36:39], v[80:83]\n"
"s_mov_b32 m0, s68\n"
"v_fma_f32 v98, v98, v10, v18\n"
"v_mfma_f32_16x16x32_bf16 v[84:87], a[36:39], v[36:39], v[84:87]\n"
"v_fma_f32 v99, v99, v11, v19\n"
"v_mfma_f32_16x16x32_bf16 v[72:75], a[40:43], v[36:39], v[72:75]\n"
"v_fma_f32 v100, v100, v12, v20\n"
"v_mfma_f32_16x16x32_bf16 v[76:79], a[44:47], v[36:39], v[76:79]\n"
"s_waitcnt lgkmcnt(2)\n"
"v_mfma_f32_16x16x32_bf16 v[88:91], a[48:51], v[40:43], v[88:91]\n"
"ds_read_b128 v[36:39], v0 offset:2112\n"
"v_fma_f32 v101, v101, v13, v21\n"
"v_mfma_f32_16x16x32_bf16 v[92:95], a[52:55], v[40:43], v[92:95]\n"
"buffer_load_dwordx4 v2, s[36:39], s79 offen nt lds\n"
"v_mfma_f32_16x16x32_bf16 v[80:83], a[56:59], v[40:43], v[80:83]\n"
"v_fma_f32 v102, v102, v14, v22\n"
"v_mfma_f32_16x16x32_bf16 v[84:87], a[60:63], v[40:43], v[84:87]\n"
"v_fma_f32 v103, v103, v15, v23\n"
"v_mfma_f32_16x16x32_bf16 v[72:75], a[64:67], v[40:43], v[72:75]\n"
"s_add_u32 m0, s68, 8256\n"
"v_mfma_f32_16x16x32_bf16 v[76:79], a[68:71], v[40:43], v[76:79]\n"
"s_waitcnt lgkmcnt(2)\n"
"v_mfma_f32_16x16x32_bf16 v[88:91], a[72:75], v[44:47], v[88:91]\n"
"ds_read_b128 v[40:43], v0 offset:3072\n"
"v_mfma_f32_16x16x32_bf16 v[92:95], a[76:79], v[44:47], v[92:95]\n"
"buffer_load_dwordx4 v2, s[40:43], s79 offen nt lds\n"
"v_mfma_f32_16x16x32_bf16 v[80:83], a[80:83], v[44:47], v[80:83]\n"
"v_exp_f32 v24, v96\n"
"v_mfma_f32_16x16x32_bf16 v[84:87], a[84:87], v[44:47], v[84:87]\n"
"v_exp_f32 v25, v97\n"
"v_mfma_f32_16x16x32_bf16 v[72:75], a[88:91], v[44:47], v[72:75]\n"
"v_exp_f32 v26, v98\n"
"v_mfma_f32_16x16x32_bf16 v[76:79], a[92:95], v[44:47], v[76:79]\n"
"s_waitcnt lgkmcnt(2)\n"
"v_mfma_f32_16x16x32_bf16 v[88:91], a[96:99], v[32:35], v[88:91]\n"
"ds_read_b128 v[44:47], v0 offset:3136\n"
"v_mfma_f32_16x16x32_bf16 v[92:95], a[100:103], v[32:35], v[92:95]\n"
"s_add_u32 s79, s79, 1024\n"
"v_mfma_f32_16x16x32_bf16 v[80:83], a[104:107], v[32:35], v[80:83]\n"
"v_exp_f32 v27, v99\n"
"v_mfma_f32_16x16x32_bf16 v[84:87], a[108:111], v[32:35], v[84:87]\n"
"s_add_u32 m0, s68, 1024\n"
"v_mfma_f32_16x16x32_bf16 v[72:75], a[112:115], v[32:35], v[72:75]\n"
"v_exp_f32 v28, v100\n"
"v_mfma_f32_16x16x32_bf16 v[76:79], a[116:119], v[32:35], v[76:79]\n"
"s_waitcnt lgkmcnt(2)\n"
"v_mfma_f32_16x16x32_bf16 v[88:91], a[120:123], v[36:39], v[88:91]\n"
"ds_read_b128 v[32:35], v0 offset:4096\n"
"v_mfma_f32_16x16x32_bf16 v[92:95], a[124:127], v[36:39], v[92:95]\n"
"buffer_load_dwordx4 v2, s[36:39], s80 offen nt lds\n"
"v_mfma_f32_16x16x32_bf16 v[80:83], a[128:131], v[36:39], v[80:83]\n"
"v_exp_f32 v29, v101\n"
"v_mfma_f32_16x16x32_bf16 v[84:87], a[132:135], v[36:39], v[84:87]\n"
"v_mfma_f32_16x16x32_bf16 v[72:75], a[136:139], v[36:39], v[72:75]\n"
"v_exp_f32 v30, v102\n"
"v_mfma_f32_16x16x32_bf16 v[76:79], a[140:143], v[36:39], v[76:79]\n"
"s_waitcnt lgkmcnt(2)\n"
"v_mfma_f32_16x16x32_bf16 v[88:91], a[144:147], v[40:43], v[88:91]\n"
"ds_read_b128 v[36:39], v0 offset:4160\n"
"s_add_u32 m0, s68, 9280\n"
"v_mfma_f32_16x16x32_bf16 v[92:95], a[148:151], v[40:43], v[92:95]\n"
"v_exp_f32 v31, v103\n"
"v_mfma_f32_16x16x32_bf16 v[80:83], a[152:155], v[40:43], v[80:83]\n"
"buffer_load_dwordx4 v2, s[40:43], s80 offen nt lds\n"
"v_mfma_f32_16x16x32_bf16 v[84:87], a[156:159], v[40:43], v[84:87]\n"
"v_mfma_f32_16x16x32_bf16 v[72:75], a[160:163], v[40:43], v[72:75]\n"
"v_fma_f32 v24, v24, s83, s83\n"
"v_mfma_f32_16x16x32_bf16 v[76:79], a[164:167], v[40:43], v[76:79]\n"
"s_waitcnt lgkmcnt(2)\n"
"v_mfma_f32_16x16x32_bf16 v[88:91], a[168:171], v[44:47], v[88:91]\n"
"ds_read_b128 v[40:43], v0 offset:5120\n"
"v_fma_f32 v25, v25, s83, s83\n"
"v_mfma_f32_16x16x32_bf16 v[92:95], a[172:175], v[44:47], v[92:95]\n"
"v_fma_f32 v26, v26, s83, s83\n"
"v_mfma_f32_16x16x32_bf16 v[80:83], a[176:179], v[44:47], v[80:83]\n"
"s_add_u32 s80, s80, 1024\n"
"v_mfma_f32_16x16x32_bf16 v[84:87], a[180:183], v[44:47], v[84:87]\n"
"v_fma_f32 v27, v27, s83, s83\n"
"v_mfma_f32_16x16x32_bf16 v[72:75], a[184:187], v[44:47], v[72:75]\n"
"s_add_u32 s68, s68, 16448\n"
"v_fma_f32 v28, v28, s83, s83\n"
"v_mfma_f32_16x16x32_bf16 v[76:79], a[188:191], v[44:47], v[76:79]\n"
"s_waitcnt lgkmcnt(2)\n"
"v_mfma_f32_16x16x32_bf16 v[88:91], a[192:195], v[32:35], v[88:91]\n"
"ds_read_b128 v[44:47], v0 offset:5184\n"
"v_fma_f32 v29, v29, s83, s83\n"
"v_mfma_f32_16x16x32_bf16 v[92:95], a[196:199], v[32:35], v[92:95]\n"
"s_cmp_eq_u32 s68, s69\ns_cselect_b32 s68, s81, s68\n"
"v_mfma_f32_16x16x32_bf16 v[80:83], a[200:203], v[32:35], v[80:83]\n"
"v_fma_f32 v30, v30, s83, s83\n"
"v_mfma_f32_16x16x32_bf16 v[84:87], a[204:207], v[32:35], v[84:87]\n"
"v_fma_f32 v31, v31, s83, s83\n"
"v_mfma_f32_16x16x32_bf16 v[72:75], a[208:211], v[32:35], v[72:75]\n"
"v_rcp_f32 v24, v24\n"
"v_mfma_f32_16x16x32_bf16 v[76:79], a[212:215], v[32:35], v[76:79]\n"
"s_waitcnt lgkmcnt(2)\n"
"v_mfma_f32_16x16x32_bf16 v[88:91], a[216:219], v[36:39], v[88:91]\n"
"ds_read_b128 v[32:35], v0 offset:6144\n"
"v_mfma_f32_16x16x32_bf16 v[92:95], a[220:223], v[36:39], v[92:95]\n"
"v_rcp_f32 v25, v25\n"
"v_mfma_f32_16x16x32_bf16 v[80:83], a[224:227], v[36:39], v[80:83]\n"
"v_mfma_f32_16x16x32_bf16 v[84:87], a[228:231], v[36:39], v[84:87]\n"
"v_rcp_f32 v26, v26\n"
"v_mfma_f32_16x16x32_bf16 v[72:75], a[232:235], v[36:39], v[72:75]\n"
"v_mfma_f32_16x16x32_bf16 v[76:79], a[236:239], v[36:39], v[76:79]\n"
"s_waitcnt lgkmcnt(2)\n"
"v_mfma_f32_16x16x32_bf16 v[88:91], a[240:243], v[40:43], v[88:91]\n"
"ds_read_b128 v[36:39], v0 offset:6208\n"
"v_mfma_f32_16x16x32_bf16 v[92:95], a[244:247], v[40:43], v[92:95]\n"
"v_rcp_f32 v27, v27\n"
"v_mfma_f32_16x16x32_bf16 v[80:83], a[248:251], v[40:43], v[80:83]\n"
"v_rcp_f32 v28, v28\n"
"v_mfma_f32_16x16x32_bf16 v[84:87], a[252:255], v[40:43], v[84:87]\n"
"v_rcp_f32 v29, v29\n"
"v_mfma_f32_16x16x32_bf16 v[72:75], v[128:131], v[40:43], v[72:75]\n"
"v_mfma_f32_16x16x32_bf16 v[76:79], v[132:135], v[40:43], v[76:79]\n"
"s_waitcnt lgkmcnt(2)\n"
"v_mfma_f32_16x16x32_bf16 v[88:91], v[136:139], v[44:47], v[88:91]\n"
"ds_read_b128 v[40:43], v0 offset:7168\n"
"v_mfma_f32_16x16x32_bf16 v[92:95], v[140:143], v[44:47], v[92:95]\n"
"v_rcp_f32 v30, v30\n"
"v_mfma_f32_16x16x32_bf16 v[80:83], v[144:147], v[44:47], v[80:83]\n"
"v_rcp_f32 v31, v31\n"
"v_mfma_f32_16x16x32_bf16 v[84:87], v[148:151], v[44:47], v[84:87]\n"
"v_mfma_f32_16x16x32_bf16 v[72:75], v[152:155], v[44:47], v[72:75]\n"
"v_mul_f32 v96, v96, v24\n"
"v_mfma_f32_16x16x32_bf16 v[76:79], v[156:159], v[44:47], v[76:79]\n"
"s_waitcnt vmcnt(12)\n"
"s_barrier\n"
"s_waitcnt lgkmcnt(2)\n"
"v_mfma_f32_16x16x32_bf16 v[88:91], v[160:163], v[32:35], v[88:91]\n"
"ds_read_b128 v[44:47], v0 offset:7232\n"
"v_mfma_f32_16x16x32_bf16 v[92:95], v[164:167], v[32:35], v[92:95]\n"
"v_add_u32 v0, s70, v1\n"
"v_mul_f32 v97, v97, v25\n"
"v_mfma_f32_16x16x32_bf16 v[80:83], v[168:171], v[32:35], v[80:83]\n"
"v_mul_f32 v98, v98, v26\n"
"v_mfma_f32_16x16x32_bf16 v[84:87], v[172:175], v[32:35], v[84:87]\n"
"v_mul_f32 v99, v99, v27\n"
"v_mfma_f32_16x16x32_bf16 v[72:75], v[176:179], v[32:35], v[72:75]\n"
"v_mul_f32 v100, v100, v28\n"
"v_mfma_f32_16x16x32_bf16 v[76:79], v[180:183], v[32:35], v[76:79]\n"
"v_mul_f32 v101, v101, v29\n"
"s_waitcnt lgkmcnt(2)\n"
"v_mfma_f32_16x16x32_bf16 v[88:91], v[184:187], v[36:39], v[88:91]\n"
"ds_read_b128 v[32:35], v0 offset:0\n"
"v_mfma_f32_16x16x32_bf16 v[92:95], v[188:191], v[36:39], v[92:95]\n"
"v_mul_f32 v102, v102, v30\n"
"v_mfma_f32_16x16x32_bf16 v[80:83], v[192:195], v[36:39], v[80:83]\n"
"v_mul_f32 v103, v103, v31\n"
"v_mfma_f32_16x16x32_bf16 v[84:87], v[196:199], v[36:39], v[84:87]\n"
"v_cvt_pk_bf16_f32 v96, v96, v97\n"
"v_mfma_f32_16x16x32_bf16 v[72:75], v[200:203], v[36:39], v[72:75]\n"
"v_cvt_pk_bf16_f32 v97, v98, v99\n"
"v_mfma_f32_16x16x32_bf16 v[76:79], v[204:207], v[36:39], v[76:79]\n"
"v_cvt_pk_bf16_f32 v98, v100, v101\n"
"s_waitcnt lgkmcnt(2)\n"
"v_mfma_f32_16x16x32_bf16 v[88:91], v[208:211], v[40:43], v[88:91]\n"
"ds_read_b128 v[36:39], v0 offset:64\n"
"v_mfma_f32_16x16x32_bf16 v[92:95], v[212:215], v[40:43], v[92:95]\n"
"v_cvt_pk_bf16_f32 v99, v102, v103\n"
"v_mfma_f32_16x16x32_bf16 v[80:83], v[216:219], v[40:43], v[80:83]\n"
"buffer_store_dwordx4 v[96:99], v3, s[56:59], s82 offen offset:0 sc1 nt\n"
"v_mfma_f32_16x16x32_bf16 v[84:87], v[220:223], v[40:43], v[84:87]\n"
"v_mfma_f32_16x16x32_bf16 v[72:75], v[224:227], v[40:43], v[72:75]\n"
"v_mfma_f32_16x16x32_bf16 v[76:79], v[228:231], v[40:43], v[76:79]\n"
"s_waitcnt lgkmcnt(2)\n"
"v_mfma_f32_16x16x32_bf16 v[88:91], v[232:235], v[44:47], v[88:91]\n"
"ds_read_b128 v[40:43], v0 offset:1024\n"
"v_mfma_f32_16x16x32_bf16 v[92:95], v[236:239], v[44:47], v[92:95]\n"
"v_mfma_f32_16x16x32_bf16 v[80:83], v[240:243], v[44:47], v[80:83]\n"
"v_mfma_f32_16x16x32_bf16 v[84:87], v[244:247], v[44:47], v[84:87]\n"
"v_mfma_f32_16x16x32_bf16 v[72:75], v[248:251], v[44:47], v[72:75]\n"
"v_mfma_f32_16x16x32_bf16 v[76:79], v[252:255], v[44:47], v[76:79]\n"
"s_sub_u32 s71, s71, 1\n"
"s_cmp_lg_u32 s71, 0\n"
"s_cbranch_scc1 Lloop_%=\n"
"s_waitcnt lgkmcnt(2)\n"
"v_mfma_f32_16x16x32_bf16 v[88:91], a[8:11], v[32:35], v[88:91]\n"
"ds_read_b128 v[44:47], v0 offset:1088\n"
"v_mfma_f32_16x16x32_bf16 v[92:95], a[12:15], v[32:35], v[92:95]\n"
"v_mfma_f32_16x16x32_bf16 v[80:83], a[16:19], v[32:35], v[80:83]\n"
"v_mfma_f32_16x16x32_bf16 v[84:87], a[20:23], v[32:35], v[84:87]\n"
"s_waitcnt lgkmcnt(2)\n"
"v_mfma_f32_16x16x32_bf16 v[88:91], a[32:35], v[36:39], v[88:91]\n"
"ds_read_b128 v[32:35], v0 offset:2048\n"
"v_mfma_f32_16x16x32_bf16 v[92:95], a[36:39], v[36:39], v[92:95]\n"
"v_mfma_f32_16x16x32_bf16 v[80:83], a[40:43], v[36:39], v[80:83]\n"
"v_fma_f32 v72, v72, v8, v16\n"
"v_fma_f32 v73, v73, v9, v17\n"
"v_mfma_f32_16x16x32_bf16 v[84:87], a[44:47], v[36:39], v[84:87]\n"
"v_fma_f32 v74, v74, v10, v18\n"
"s_waitcnt lgkmcnt(2)\n"
"v_mfma_f32_16x16x32_bf16 v[88:91], a[56:59], v[40:43], v[88:91]\n"
"ds_read_b128 v[36:39], v0 offset:2112\n"
"v_fma_f32 v75, v75, v11, v19\n"
"v_mfma_f32_16x16x32_bf16 v[92:95], a[60:63], v[40:43], v[92:95]\n"
"v_fma_f32 v76, v76, v12, v20\n"
"v_mfma_f32_16x16x32_bf16 v[80:83], a[64:67], v[40:43], v[80:83]\n"
"v_fma_f32 v77, v77, v13, v21\n"
"v_fma_f32 v78, v78, v14, v22\n"
"v_mfma_f32_16x16x32_bf16 v[84:87], a[68:71], v[40:43], v[84:87]\n"
"s_waitcnt lgkmcnt(2)\n"
"v_mfma_f32_16x16x32_bf16 v[88:91], a[80:83], v[44:47], v[88:91]\n"
"ds_read_b128 v[40:43], v0 offset:3072\n"
"v_fma_f32 v79, v79, v15, v23\n"
"v_mfma_f32_16x16x32_bf16 v[92:95], a[84:87], v[44:47], v[92:95]\n"
"v_exp_f32 v24, v72\n"
"v_mfma_f32_16x16x32_bf16 v[80:83], a[88:91], v[44:47], v[80:83]\n"
"v_exp_f32 v25, v73\n"
"v_mfma_f32_16x16x32_bf16 v[84:87], a[92:95], v[44:47], v[84:87]\n"
"s_waitcnt lgkmcnt(2)\n"
"v_mfma_f32_16x16x32_bf16 v[88:91], a[104:107], v[32:35], v[88:91]\n"
"ds_read_b128 v[44:47], v0 offset:3136\n"
"v_mfma_f32_16x16x32_bf16 v[92:95], a[108:111], v[32:35], v[92:95]\n"
"v_exp_f32 v26, v74\n"
"v_mfma_f32_16x16x32_bf16 v[80:83], a[112:115], v[32:35], v[80:83]\n"
"v_exp_f32 v27, v75\n"
"v_mfma_f32_16x16x32_bf16 v[84:87], a[116:119], v[32:35], v[84:87]\n"
"s_waitcnt lgkmcnt(2)\n"
"v_mfma_f32_16x16x32_bf16 v[88:91], a[128:131], v[36:39], v[88:91]\n"
"ds_read_b128 v[32:35], v0 offset:4096\n"
"v_mfma_f32_16x16x32_bf16 v[92:95], a[132:135], v[36:39], v[92:95]\n"
"v_exp_f32 v28, v76\n"
"v_mfma_f32_16x16x32_bf16 v[80:83], a[136:139], v[36:39], v[80:83]\n"
"v_exp_f32 v29, v77\n"
"v_mfma_f32_16x16x32_bf16 v[84:87], a[140:143], v[36:39], v[84:87]\n"
"s_waitcnt lgkmcnt(2)\n"
"v_mfma_f32_16x16x32_bf16 v[88:91], a[152:155], v[40:43], v[88:91]\n"
"ds_read_b128 v[36:39], v0 offset:4160\n"
"v_mfma_f32_16x16x32_bf16 v[92:95], a[156:159], v[40:43], v[92:95]\n"
"v_exp_f32 v30, v78\n"
"v_mfma_f32_16x16x32_bf16 v[80:83], a[160:163], v[40:43], v[80:83]\n"
"v_exp_f32 v31, v79\n"
"v_mfma_f32_16x16x32_bf16 v[84:87], a[164:167], v[40:43], v[84:87]\n"
"v_fma_f32 v24, v24, s83, s83\n"
"s_waitcnt lgkmcnt(2)\n"
"v_mfma_f32_16x16x32_bf16 v[88:91], a[176:179], v[44:47], v[88:91]\n"
"ds_read_b128 v[40:43], v0 offset:5120\n"
"v_fma_f32 v25, v25, s83, s83\n"
"v_mfma_f32_16x16x32_bf16 v[92:95], a[180:183], v[44:47], v[92:95]\n"
"v_fma_f32 v26, v26, s83, s83\n"
"v_fma_f32 v27, v27, s83, s83\n"
"v_mfma_f32_16x16x32_bf16 v[80:83], a[184:187], v[44:47], v[80:83]\n"
"v_fma_f32 v28, v28, s83, s83\n"
"v_fma_f32 v29, v29, s83, s83\n"
"v_mfma_f32_16x16x32_bf16 v[84:87], a[188:191], v[44:47], v[84:87]\n"
"v_fma_f32 v30, v30, s83, s83\n"
"s_waitcnt lgkmcnt(2)\n"
"v_mfma_f32_16x16x32_bf16 v[88:91], a[200:203], v[32:35], v[88:91]\n"
"ds_read_b128 v[44:47], v0 offset:5184\n"
"v_mfma_f32_16x16x32_bf16 v[92:95], a[204:207], v[32:35], v[92:95]\n"
"v_fma_f32 v31, v31, s83, s83\n"
"v_mfma_f32_16x16x32_bf16 v[80:83], a[208:211], v[32:35], v[80:83]\n"
"v_rcp_f32 v24, v24\n"
"v_mfma_f32_16x16x32_bf16 v[84:87], a[212:215], v[32:35], v[84:87]\n"
"s_waitcnt lgkmcnt(2)\n"
"v_mfma_f32_16x16x32_bf16 v[88:91], a[224:227], v[36:39], v[88:91]\n"
"ds_read_b128 v[32:35], v0 offset:6144\n"
"v_mfma_f32_16x16x32_bf16 v[92:95], a[228:231], v[36:39], v[92:95]\n"
"v_rcp_f32 v25, v25\n"
"v_mfma_f32_16x16x32_bf16 v[80:83], a[232:235], v[36:39], v[80:83]\n"
"v_rcp_f32 v26, v26\n"
"v_mfma_f32_16x16x32_bf16 v[84:87], a[236:239], v[36:39], v[84:87]\n"
"s_waitcnt lgkmcnt(2)\n"
"v_mfma_f32_16x16x32_bf16 v[88:91], a[248:251], v[40:43], v[88:91]\n"
"ds_read_b128 v[36:39], v0 offset:6208\n"
"v_mfma_f32_16x16x32_bf16 v[92:95], a[252:255], v[40:43], v[92:95]\n"
"v_rcp_f32 v27, v27\n"
"v_mfma_f32_16x16x32_bf16 v[80:83], v[128:131], v[40:43], v[80:83]\n"
"v_rcp_f32 v28, v28\n"
"v_mfma_f32_16x16x32_bf16 v[84:87], v[132:135], v[40:43], v[84:87]\n"
"s_waitcnt lgkmcnt(2)\n"
"v_mfma_f32_16x16x32_bf16 v[88:91], v[144:147], v[44:47], v[88:91]\n"
"ds_read_b128 v[40:43], v0 offset:7168\n"
"v_mfma_f32_16x16x32_bf16 v[92:95], v[148:151], v[44:47], v[92:95]\n"
"v_rcp_f32 v29, v29\n"
"v_mfma_f32_16x16x32_bf16 v[80:83], v[152:155], v[44:47], v[80:83]\n"
"v_rcp_f32 v30, v30\n"
"v_mfma_f32_16x16x32_bf16 v[84:87], v[156:159], v[44:47], v[84:87]\n"
"s_waitcnt lgkmcnt(2)\n"
"v_mfma_f32_16x16x32_bf16 v[88:91], v[168:171], v[32:35], v[88:91]\n"
"ds_read_b128 v[44:47], v0 offset:7232\n"
"v_mfma_f32_16x16x32_bf16 v[92:95], v[172:175], v[32:35], v[92:95]\n"
"v_rcp_f32 v31, v31\n"
"v_mfma_f32_16x16x32_bf16 v[80:83], v[176:179], v[32:35], v[80:83]\n"
"v_mul_f32 v72, v72, v24\n"
"v_mul_f32 v73, v73, v25\n"
"v_mfma_f32_16x16x32_bf16 v[84:87], v[180:183], v[32:35], v[84:87]\n"
"v_mul_f32 v74, v74, v26\n"
"s_waitcnt lgkmcnt(2)\n"
"v_mfma_f32_16x16x32_bf16 v[88:91], v[192:195], v[36:39], v[88:91]\n"
"v_mul_f32 v75, v75, v27\n"
"v_mul_f32 v76, v76, v28\n"
"v_mfma_f32_16x16x32_bf16 v[92:95], v[196:199], v[36:39], v[92:95]\n"
"v_mul_f32 v77, v77, v29\n"
"v_mul_f32 v78, v78, v30\n"
"v_mfma_f32_16x16x32_bf16 v[80:83], v[200:203], v[36:39], v[80:83]\n"
"v_mul_f32 v79, v79, v31\n"
"v_cvt_pk_bf16_f32 v72, v72, v73\n"
"v_mfma_f32_16x16x32_bf16 v[84:87], v[204:207], v[36:39], v[84:87]\n"
"v_cvt_pk_bf16_f32 v73, v74, v75\n"
"s_waitcnt lgkmcnt(1)\n"
"v_mfma_f32_16x16x32_bf16 v[88:91], v[216:219], v[40:43], v[88:91]\n"
"v_cvt_pk_bf16_f32 v74, v76, v77\n"
"v_cvt_pk_bf16_f32 v75, v78, v79\n"
"v_mfma_f32_16x16x32_bf16 v[92:95], v[220:223], v[40:43], v[92:95]\n"
"s_waitcnt vmcnt(0)\n"
"buffer_store_dwordx4 v[72:75], v3, s[56:59], s82 offen offset:1024 sc1 nt\n"
"v_mfma_f32_16x16x32_bf16 v[80:83], v[224:227], v[40:43], v[80:83]\n"
"v_mfma_f32_16x16x32_bf16 v[84:87], v[228:231], v[40:43], v[84:87]\n"
"s_waitcnt lgkmcnt(0)\n"
"v_mfma_f32_16x16x32_bf16 v[88:91], v[240:243], v[44:47], v[88:91]\n"
"v_mfma_f32_16x16x32_bf16 v[92:95], v[244:247], v[44:47], v[92:95]\n"
"v_mfma_f32_16x16x32_bf16 v[80:83], v[248:251], v[44:47], v[80:83]\n"
"v_mfma_f32_16x16x32_bf16 v[84:87], v[252:255], v[44:47], v[84:87]\n"
"s_nop 6\n"
"v_fma_f32 v80, v80, v8, v16\n"
"v_fma_f32 v81, v81, v9, v17\n"
"v_fma_f32 v82, v82, v10, v18\n"
"v_fma_f32 v83, v83, v11, v19\n"
"v_fma_f32 v84, v84, v12, v20\n"
"v_fma_f32 v85, v85, v13, v21\n"
"v_fma_f32 v86, v86, v14, v22\n"
"v_fma_f32 v87, v87, v15, v23\n"
"v_exp_f32 v24, v80\n"
"v_exp_f32 v25, v81\n"
"v_exp_f32 v26, v82\n"
"v_exp_f32 v27, v83\n"
"v_exp_f32 v28, v84\n"
"v_exp_f32 v29, v85\n"
"v_exp_f32 v30, v86\n"
"v_exp_f32 v31, v87\n"
"v_fma_f32 v24, v24, s83, s83\n"
"v_fma_f32 v25, v25, s83, s83\n"
"v_fma_f32 v26, v26, s83, s83\n"
"v_fma_f32 v27, v27, s83, s83\n"
"v_fma_f32 v28, v28, s83, s83\n"
"v_fma_f32 v29, v29, s83, s83\n"
"v_fma_f32 v30, v30, s83, s83\n"
"v_fma_f32 v31, v31, s83, s83\n"
"v_rcp_f32 v24, v24\n"
"v_rcp_f32 v25, v25\n"
"v_rcp_f32 v26, v26\n"
"v_rcp_f32 v27, v27\n"
"v_rcp_f32 v28, v28\n"
"v_rcp_f32 v29, v29\n"
"v_rcp_f32 v30, v30\n"
"v_rcp_f32 v31, v31\n"
"v_mul_f32 v80, v80, v24\n"
"v_mul_f32 v81, v81, v25\n"
"v_mul_f32 v82, v82, v26\n"
"v_mul_f32 v83, v83, v27\n"
"v_mul_f32 v84, v84, v28\n"
"v_mul_f32 v85, v85, v29\n"
"v_mul_f32 v86, v86, v30\n"
"v_mul_f32 v87, v87, v31\n"
"v_cvt_pk_bf16_f32 v80, v80, v81\n"
"v_cvt_pk_bf16_f32 v81, v82, v83\n"
"v_cvt_pk_bf16_f32 v82, v84, v85\n"
"v_cvt_pk_bf16_f32 v83, v86, v87\n"
"buffer_store_dwordx4 v[80:83], v3, s[56:59], s82 offen offset:2048 sc1 nt\n"
"v_fma_f32 v88, v88, v8, v16\n"
"v_fma_f32 v89, v89, v9, v17\n"
"v_fma_f32 v90, v90, v10, v18\n"
"v_fma_f32 v91, v91, v11, v19\n"
"v_fma_f32 v92, v92, v12, v20\n"
"v_fma_f32 v93, v93, v13, v21\n"
"v_fma_f32 v94, v94, v14, v22\n"
"v_fma_f32 v95, v95, v15, v23\n"
"v_exp_f32 v24, v88\n"
"v_exp_f32 v25, v89\n"
"v_exp_f32 v26, v90\n"
"v_exp_f32 v27, v91\n"
"v_exp_f32 v28, v92\n"
"v_exp_f32 v29, v93\n"
"v_exp_f32 v30, v94\n"
"v_exp_f32 v31, v95\n"
"v_fma_f32 v24, v24, s83, s83\n"
"v_fma_f32 v25, v25, s83, s83\n"
"v_fma_f32 v26, v26, s83, s83\n"
"v_fma_f32 v27, v27, s83, s83\n"
"v_fma_f32 v28, v28, s83, s83\n"
"v_fma_f32 v29, v29, s83, s83\n"
"v_fma_f32 v30, v30, s83, s83\n"
"v_fma_f32 v31, v31, s83, s83\n"
"v_rcp_f32 v24, v24\n"
"v_rcp_f32 v25, v25\n"
"v_rcp_f32 v26, v26\n"
"v_rcp_f32 v27, v27\n"
"v_rcp_f32 v28, v28\n"
"v_rcp_f32 v29, v29\n"
"v_rcp_f32 v30, v30\n"
"v_rcp_f32 v31, v31\n"
"v_mul_f32 v88, v88, v24\n"
"v_mul_f32 v89, v89, v25\n"
"v_mul_f32 v90, v90, v26\n"
"v_mul_f32 v91, v91, v27\n"
"v_mul_f32 v92, v92, v28\n"
"v_mul_f32 v93, v93, v29\n"
"v_mul_f32 v94, v94, v30\n"
"v_mul_f32 v95, v95, v31\n"
"v_cvt_pk_bf16_f32 v88, v88, v89\n"
"v_cvt_pk_bf16_f32 v89, v90, v91\n"
"v_cvt_pk_bf16_f32 v90, v92, v93\n"
"v_cvt_pk_bf16_f32 v91, v94, v95\n"
"buffer_store_dwordx4 v[88:91], v3, s[56:59], s82 offen offset:3072 sc1 nt\n"
"Lpf_end_%=:\n"
  : "+v"(tid)
  : [bid] "s"(bid), [lds] "s"(lds), [karg] "s"(karg)
  : "v1","v2","v3","v4","v5","v6","v7","v8","v9","v10","v11","v12","v13","v14","v15","v16","v17","v18","v19","v20","v21","v22","v23","v24","v25","v26","v27","v28","v29","v30","v31","v32","v33","v34","v35","v36","v37","v38","v39","v40","v41","v42","v43","v44","v45","v46","v47","v48","v49","v50","v51","v52","v53","v54","v55","v56","v57","v58","v59","v60","v61","v62","v63","v64","v65","v66","v67","v68","v69","v70","v71","v72","v73","v74","v75","v76","v77","v78","v79","v80","v81","v82","v83","v84","v85","v86","v87","v88","v89","v90","v91","v92","v93","v94","v95","v96","v97","v98","v99","v100","v101","v102","v103","v104","v105","v106","v107","v108","v109","v110","v111","v112","v113","v114","v115","v116","v117","v118","v119","v120","v121","v122","v123","v124","v125","v126","v127","v128","v129","v130","v131","v132","v133","v134","v135","v136","v137","v138","v139","v140","v141","v142","v143","v144","v145","v146","v147","v148","v149","v150","v151","v152","v153","v154","v155","v156","v157","v158","v159","v160","v161","v162","v163","v164","v165","v166","v167","v168","v169","v170","v171","v172","v173","v174","v175","v176","v177","v178","v179","v180","v181","v182","v183","v184","v185","v186","v187","v188","v189","v190","v191","v192","v193","v194","v195","v196","v197","v198","v199","v200","v201","v202","v203","v204","v205","v206","v207","v208","v209","v210","v211","v212","v213","v214","v215","v216","v217","v218","v219","v220","v221","v222","v223","v224","v225","v226","v227","v228","v229","v230","v231","v232","v233","v234","v235","v236","v237","v238","v239","v240","v241","v242","v243","v244","v245","v246","v247","v248","v249","v250","v251","v252","v253","v254","v255","a0","a1","a2","a3","a4","a5","a6","a7","a8","a9","a10","a11","a12","a13","a14","a15","a16","a17","a18","a19","a20","a21","a22","a23","a24","a25","a26","a27","a28","a29","a30","a31","a32","a33","a34","a35","a36","a37","a38","a39","a40","a41","a42","a43","a44","a45","a46","a47","a48","a49","a50","a51","a52","a53","a54","a55","a56","a57","a58","a59","a60","a61","a62","a63","a64","a65","a66","a67","a68","a69","a70","a71","a72","a73","a74","a75","a76","a77","a78","a79","a80","a81","a82","a83","a84","a85","a86","a87","a88","a89","a90","a91","a92","a93","a94","a95","a96","a97","a98","a99","a100","a101","a102","a103","a104","a105","a106","a107","a108","a109","a110","a111","a112","a113","a114","a115","a116","a117","a118","a119","a120","a121","a122","a123","a124","a125","a126","a127","a128","a129","a130","a131","a132","a133","a134","a135","a136","a137","a138","a139","a140","a141","a142","a143","a144","a145","a146","a147","a148","a149","a150","a151","a152","a153","a154","a155","a156","a157","a158","a159","a160","a161","a162","a163","a164","a165","a166","a167","a168","a169","a170","a171","a172","a173","a174","a175","a176","a177","a178","a179","a180","a181","a182","a183","a184","a185","a186","a187","a188","a189","a190","a191","a192","a193","a194","a195","a196","a197","a198","a199","a200","a201","a202","a203","a204","a205","a206","a207","a208","a209","a210","a211","a212","a213","a214","a215","a216","a217","a218","a219","a220","a221","a222","a223","a224","a225","a226","a227","a228","a229","a230","a231","a232","a233","a234","a235","a236","a237","a238","a239","a240","a241","a242","a243","a244","a245","a246","a247","a248","a249","a250","a251","a252","a253","a254","a255","s16","s17","s18","s19","s20","s21","s22","s23","s24","s25","s26","s27","s28","s29","s30","s31","s36","s37","s38","s39","s40","s41","s42","s43","s44","s45","s46","s47","s48","s49","s50","s51","s52","s53","s54","s55","s56","s57","s58","s59","s60","s61","s62","s63","s64","s65","s66","s67","s68","s69","s70","s71","s72","s73","s74","s75","s76","s77","s78","s79","s80","s81","s82","s83","s84","s85","s86","s87","s88","s89","s90","s91","s92","s93","s94","s95","s96","s97","s98","s99","vcc","scc","memory");
}

extern "C" void kernel_launch(void* const* d_in, const int* in_sizes, int n_in, void* d_out, int out_size, void* d_ws, size_t ws_size,
                              hipStream_t stream) {
  conv_bn_silu_kernel<<<256, 256, 0, stream>>>(d_in[0], d_in[1], d_in[2], d_in[3], d_in[4], d_in[5], d_out, d_ws);
}
```
